```python
import torch

def _softmax(x, axis=-1):
    return torch.softmax(x, dim=axis)
def _gelu(x):
    return torch.nn.functional.gelu(x, approximate="tanh")
def _silu(x):
    return torch.nn.functional.silu(x)
def _sigmoid(x):
    return torch.sigmoid(x)
def _layernorm(x, axis=-1, eps=1e-5):
    m = x.mean(dim=axis, keepdim=True)
    v = x.var(dim=axis, keepdim=True, unbiased=False)
    return (x - m) / torch.sqrt(v + eps)
def _scatter_add(tgt, idx, src, axis=0):
    return tgt.clone().index_add_(axis, idx, src)
def _segment_sum(src, seg, n):
    out = torch.zeros((n,) + src.shape[1:], dtype=src.dtype)
    return out.index_add_(0, seg, src)
def _pad(x, pw):
    flat = tuple(v for pair in reversed(pw) for v in pair)
    return torch.nn.functional.pad(x, flat)

kernel_name = 'recurrent_scan__mha_block_cached__geglu_ffn__decode'

def setup_inputs():

    _w = (lambda a: (lambda t: t.to(torch.bfloat16) if t.is_floating_point() else t)(torch.as_tensor(a)))
    def _A_setup():
        B, Sq, D = 2, 1, 640
        H, Sk, Dh = 32, 4096, 192
        return {
            "x": _w((lambda *s: torch.randn(*s, dtype=torch.bfloat16) * 0.05)(B, Sq, D)),
            "k_cache": _w((lambda *s: torch.randn(*s, dtype=torch.bfloat16) * 1.6)(B, H, Sk, Dh)),
            "v_cache": _w((lambda *s: torch.randn(*s, dtype=torch.bfloat16) * 0.2)(B, H, Sk, Dh)),
            "wq": _w((lambda *s: torch.randn(*s, dtype=torch.bfloat16) * 1.6)(D, H * Dh) * (D ** -0.5)),
            "wo": _w((lambda *s: torch.randn(*s, dtype=torch.bfloat16) * 0.2)(H * Dh, D) * ((H * Dh) ** -0.5)),
        }
    _A_in = _A_setup()

    def _B_setup():
        B, S, D, F = 2, 1, 640, 1536
        return {
            "x": _w((lambda *s: torch.randn(*s, dtype=torch.bfloat16) * 0.05)(B, S, D)),
            "w_gate": _w((lambda *s: torch.randn(*s, dtype=torch.bfloat16) * 0.1)(D, F)),
            "w_up": _w((lambda *s: torch.randn(*s, dtype=torch.bfloat16) * 0.1)(D, F)),
            "w_down": _w((lambda *s: torch.randn(*s, dtype=torch.bfloat16) * 0.1)(F, D)),
        }
    _B_in = _B_setup()

    return {
        "A_x": _A_in["x"],
        "A_k_cache": _A_in["k_cache"],
        "A_v_cache": _A_in["v_cache"],
        "A_wq": _A_in["wq"],
        "A_wo": _A_in["wo"],
        "B_x": _B_in["x"],
        "B_w_gate": _B_in["w_gate"],
        "B_w_up": _B_in["w_up"],
        "B_w_down": _B_in["w_down"],
    }

def reference(A_x, A_k_cache, A_v_cache, A_wq, A_wo, B_x, B_w_gate, B_w_up, B_w_down):
    def _A_ref(x, k_cache, v_cache, wq, wo):
        B, Sq, D = x.shape
        H, Dh = 32, 192
        q = (lambda x, *d: x.permute(*d))((x @ wq).reshape(B, Sq, H, Dh), 0, 2, 1, 3)
        scores = torch.einsum("bhqd,bhkd->bhqk", q, k_cache) * (Dh ** -0.5)
        p = _softmax(scores, axis=-1)
        o = torch.einsum("bhqk,bhkd->bhqd", p, v_cache)
        return (lambda x, *d: x.permute(*d))(o, 0, 2, 1, 3).reshape(B, Sq, H * Dh) @ wo

    def _B_ref(x, w_gate, w_up, w_down):
        return (_gelu(x @ w_gate) * (x @ w_up)) @ w_down

    x = A_x
    xn = _layernorm(x, axis=-1)
    s = _A_ref(xn, A_k_cache, A_v_cache, A_wq, A_wo)
    g = _silu(_B_ref(xn, B_w_gate, B_w_up, B_w_down))
    return x + s * g
```

```cpp
#include <hip/hip_runtime.h>
#include <hip/hip_bf16.h>
#include <stdint.h>

typedef unsigned short u16;
typedef unsigned v4u __attribute__((ext_vector_type(4)));
constexpr int B_=2, D_=640, H_=32, SK=4096, DH=192, F_=1536;
constexpr int NSPLIT=4;

__device__ __forceinline__ float bf2f(u16 v){ return __uint_as_float(((unsigned)v)<<16); }
__device__ __forceinline__ u16 f2bf(float f){ __hip_bfloat16 h = __float2bfloat16(f); return *reinterpret_cast<u16*>(&h); }
__device__ __forceinline__ float rbf(float f){ return bf2f(f2bf(f)); }

__global__ void __launch_bounds__(256) k1(const u16* x, const u16* wq, const u16* wg, const u16* wu, float* q, float* hid){
  __shared__ float xn[2][D_];
  __shared__ float red[2][2];
  int t=threadIdx.x;
  if(t<128){
    int b=t>>6, l=t&63; float s=0;
    for(int i=l;i<D_;i+=64) s+=bf2f(x[b*D_+i]);
    for(int o=32;o;o>>=1) s+=__shfl_xor(s,o);
    float m=s/D_; float v=0;
    for(int i=l;i<D_;i+=64){ float d=bf2f(x[b*D_+i])-m; v+=d*d; }
    for(int o=32;o;o>>=1) v+=__shfl_xor(v,o);
    v/=D_;
    float mb=rbf(m), vb=rbf(v);
    float den=rbf(sqrtf(rbf(vb+1e-5f)));
    for(int i=l;i<D_;i+=64) xn[b][i]=rbf(rbf(bf2f(x[b*D_+i])-mb)/den);
  }
  __syncthreads();
  int c=blockIdx.x*256+t;
  if(c<H_*DH){
    float a0=0,a1=0;
    for(int k=0;k<D_;k++){ float w=bf2f(wq[k*(H_*DH)+c]); a0+=xn[0][k]*w; a1+=xn[1][k]*w; }
    q[c]=rbf(a0); q[H_*DH+c]=rbf(a1);
  } else {
    c-=H_*DH;
    float g0=0,g1=0,u0=0,u1=0;
    for(int k=0;k<D_;k++){ float w=bf2f(wg[k*F_+c]); float w2=bf2f(wu[k*F_+c]); g0+=xn[0][k]*w; g1+=xn[1][k]*w; u0+=xn[0][k]*w2; u1+=xn[1][k]*w2; }
    g0=rbf(g0); g1=rbf(g1); u0=rbf(u0); u1=rbf(u1);
    float ge0=rbf(0.5f*g0*(1.f+tanhf(0.7978845608f*(g0+0.044715f*g0*g0*g0))));
    float ge1=rbf(0.5f*g1*(1.f+tanhf(0.7978845608f*(g1+0.044715f*g1*g1*g1))));
    hid[c]=rbf(ge0*u0); hid[F_+c]=rbf(ge1*u1);
  }
}
__global__ void __launch_bounds__(256) k2(const u16* K, const u16* V, const float* q, float* po, float* pml){
  __shared__ float sc[1024];
  __shared__ float qs[DH];
  __shared__ float red[8];
  __shared__ float oacc[32][DH];
  int bh=blockIdx.x/NSPLIT, s=blockIdx.x%NSPLIT, t=threadIdx.x;
  if(t<DH) qs[t]=q[bh*DH+t];
  __syncthreads();
  int r=t>>3, c=t&7;
  const u16* kb=K+((size_t)bh*SK+s*1024)*DH;
  for(int g=0;g<32;g++){
    int key=g*32+r; float a=0;
    for(int i=0;i<3;i++){
      v4u w=*reinterpret_cast<const v4u*>(kb+(size_t)key*DH+i*64+c*8);
      for(int j=0;j<4;j++){ a+=__uint_as_float(w[j]<<16)*qs[i*64+c*8+2*j]+__uint_as_float(w[j]&0xffff0000u)*qs[i*64+c*8+2*j+1]; }
    }
    a+=__shfl_xor(a,1); a+=__shfl_xor(a,2); a+=__shfl_xor(a,4);
    if(c==0) sc[key]=a*0.07216878364870322f;
  }
  __syncthreads();
  float m=-INFINITY;
  for(int i=t;i<1024;i+=256) m=fmaxf(m,sc[i]);
  for(int o=32;o;o>>=1) m=fmaxf(m,__shfl_xor(m,o));
  if((t&63)==0) red[t>>6]=m;
  __syncthreads();
  m=fmaxf(fmaxf(red[0],red[1]),fmaxf(red[2],red[3]));
  float l=0;
  for(int i=t;i<1024;i+=256){ float p=__expf(sc[i]-m); sc[i]=p; l+=p; }
  for(int o=32;o;o>>=1) l+=__shfl_xor(l,o);
  if((t&63)==0) red[4+(t>>6)]=l;
  __syncthreads();
  l=red[4]+red[5]+red[6]+red[7];
  const u16* vb=V+((size_t)bh*SK+s*1024)*DH;
  float acc[24];
  for(int i=0;i<24;i++) acc[i]=0;
  for(int g=0;g<32;g++){
    int key=g*32+r; float p=sc[key];
    #pragma unroll
    for(int i=0;i<3;i++){
      v4u w=*reinterpret_cast<const v4u*>(vb+(size_t)key*DH+i*64+c*8);
      #pragma unroll
      for(int j=0;j<4;j++){ acc[i*8+2*j]+=p*__uint_as_float(w[j]<<16); acc[i*8+2*j+1]+=p*__uint_as_float(w[j]&0xffff0000u); }
    }
  }
  #pragma unroll
  for(int i=0;i<3;i++)
  #pragma unroll
  for(int j=0;j<8;j++) oacc[r][i*64+c*8+j]=acc[i*8+j];
  __syncthreads();
  if(t<DH){ float o=0; for(int i=0;i<32;i++) o+=oacc[i][t]; po[blockIdx.x*DH+t]=o; }
  if(t==0){ pml[blockIdx.x*2]=m; pml[blockIdx.x*2+1]=l; }
}
__global__ void __launch_bounds__(256) k3(const u16* x, const u16* wo, const u16* wd, const float* po, const float* pml, const float* hid, u16* out){
  __shared__ float wgt[256];
  __shared__ float red[4][32];
  int t=threadIdx.x;
  { int bh=t>>2; float M=-INFINITY; for(int s=0;s<4;s++) M=fmaxf(M,pml[(bh*4+s)*2]);
    float L=0; for(int s=0;s<4;s++) L+=pml[(bh*4+s)*2+1]*__expf(pml[(bh*4+s)*2]-M);
    wgt[t]=__expf(pml[t*2]-M)/L; }
  __syncthreads();
  int c0=blockIdx.x*8;
  float a[2][8], y[2][8];
  for(int j=0;j<8;j++){a[0][j]=a[1][j]=y[0][j]=y[1][j]=0;}
  for(int k=t;k<H_*DH;k+=256){
    int h=k/DH, d=k%DH; float o[2];
    for(int b=0;b<2;b++){ float v=0; for(int s=0;s<4;s++){ int p=(b*H_+h)*4+s; v+=wgt[p]*po[p*DH+d]; } o[b]=rbf(v); }
    v4u w=*reinterpret_cast<const v4u*>(wo+(size_t)k*D_+c0);
    #pragma unroll
    for(int j=0;j<4;j++){ float lo=__uint_as_float(w[j]<<16), hi=__uint_as_float(w[j]&0xffff0000u);
      a[0][2*j]+=o[0]*lo; a[0][2*j+1]+=o[0]*hi; a[1][2*j]+=o[1]*lo; a[1][2*j+1]+=o[1]*hi; }
  }
  for(int k=t;k<F_;k+=256){
    float h0=hid[k], h1=hid[F_+k];
    v4u w=*reinterpret_cast<const v4u*>(wd+(size_t)k*D_+c0);
    #pragma unroll
    for(int j=0;j<4;j++){ float lo=__uint_as_float(w[j]<<16), hi=__uint_as_float(w[j]&0xffff0000u);
      y[0][2*j]+=h0*lo; y[0][2*j+1]+=h0*hi; y[1][2*j]+=h1*lo; y[1][2*j+1]+=h1*hi; }
  }
  #pragma unroll
  for(int b=0;b<2;b++)
  #pragma unroll
  for(int j=0;j<8;j++){ float v=a[b][j], u=y[b][j];
    for(int o=32;o;o>>=1){ v+=__shfl_xor(v,o); u+=__shfl_xor(u,o);}
    if((t&63)==0){ red[t>>6][b*8+j]=v; red[t>>6][16+b*8+j]=u; } }
  __syncthreads();
  if(t<16){ int b=t>>3,j=t&7; float s=rbf(red[0][t]+red[1][t]+red[2][t]+red[3][t]);
    float yy=rbf(red[0][16+t]+red[1][16+t]+red[2][16+t]+red[3][16+t]);
    float g=rbf(yy/(1.f+__expf(-yy)));
    out[b*D_+c0+j]=f2bf(bf2f(x[b*D_+c0+j])+rbf(s*g)); }
}

extern "C" void kernel_launch(void* const* d_in, const int* in_sizes, int n_in,
                              void* d_out, int out_size, void* d_ws, size_t ws_size,
                              hipStream_t stream) {
  const u16* x=(const u16*)d_in[0]; const u16* K=(const u16*)d_in[1]; const u16* V=(const u16*)d_in[2];
  const u16* wq=(const u16*)d_in[3]; const u16* wo=(const u16*)d_in[4];
  const u16* wg=(const u16*)d_in[6]; const u16* wu=(const u16*)d_in[7]; const u16* wd=(const u16*)d_in[8];
  float* ws=(float*)d_ws;
  float* q=ws; float* hid=q+2*H_*DH; float* po=hid+2*F_; float* pml=po+256*DH;
  k1<<<(H_*DH+F_)/256,256,0,stream>>>(x,wq,wg,wu,q,hid);
  k2<<<256,256,0,stream>>>(K,V,q,po,pml);
  k3<<<80,256,0,stream>>>(x,wo,wd,po,pml,hid,(u16*)d_out);
}
```

```cpp
#include <hip/hip_runtime.h>
#include <stdint.h>
#include <type_traits>

typedef unsigned u32;
typedef unsigned long long u64;
typedef unsigned short u16;
typedef u32 v4u __attribute__((ext_vector_type(4)));
typedef u32 v2u __attribute__((ext_vector_type(2)));
typedef float v4f __attribute__((ext_vector_type(4)));
typedef short s4 __attribute__((ext_vector_type(4)));
typedef __bf16 bf8 __attribute__((ext_vector_type(8)));
typedef __bf16 bf2 __attribute__((ext_vector_type(2)));
typedef __amdgpu_buffer_rsrc_t rsrc_t;

constexpr int D_ = 640, H_ = 32, SK = 4096, DH = 192, F_ = 1536, HD = H_ * DH;
constexpr int AUX_NT = 2, AUX_SC1 = 16;
constexpr u32 OOB = 0x80000000u;
constexpr u32 SENT = 0xFFFFFFFFu;
constexpr int SPIN_CAP = 1 << 17;

constexpr int WS_E = 0;
constexpr int REC = 896, R_O = 256, R_ML = R_O + 768;
constexpr int WS_Q = R_O + 256 * REC;
constexpr int WS_F = WS_Q + 64 * 1536;
constexpr int WS_Y = WS_F + 48 * 1024;
constexpr int WS_S = WS_Y + 240 * 512;
#define PSTR 32
#define PAUX AUX_SC1
constexpr int WS_P = WS_S + 240 * 512;
constexpr int WS_END = WS_P + 16 * 16 * PSTR;

#define NPRE 14
#define WMIN 6
#define QT 410
#define FT 820
#define GSTEP 49
#define TAU 665
#define TOFF 190
#define XV 14
#define N0 6
#define CC 40
#define WE 4
#define WOT 45
#define S0 8
#define S1 12
#define S2 12
#define MARG0 0
#define KEEP1 28
#define KEEP 18
#define LMAX 46
#define GK 26
#define GV 14
#define LV 26
#define SIG 20
#define REP1 22
#define UPOS 0
constexpr int CK0 = 4, CKS = 4, NCK = 14, CKD = 7, RLEAD = 3, C0 = 2, LATE = 90, MISS = 130, RAMP = 16, REST = 5, USL = 8, ROT = 7, NQ = 8;

#define IC(n) std::integral_constant<int, (n)>{}
#define STAMP(i)

constexpr int L_XN = 0;
constexpr int L_Q = L_XN + 4 * 2560;
constexpr int L_H = L_Q + 384;
constexpr int L_ML = L_H + 256;
constexpr int L_FQ = L_ML + 32, L_FP = L_ML + 36, L_CNT = L_ML + 40, L_FW = L_ML + 44;
constexpr int L_TB = L_ML + 64;
constexpr int L_RO = L_TB + 256;
constexpr int L_RS = L_RO + 8 * 768;
constexpr int L_SP = L_RS + 9 * 1024;
constexpr int L_TS = L_SP + 2048;
constexpr int L_END = L_TS + 128;

template <int I, int N, class F> __device__ __forceinline__ void sfor(F&& f) {
  if constexpr (I < N) { f(std::integral_constant<int, I>{}); sfor<I + 1, N>(f); }
}
#define PIN() do { asm volatile("" ::: "memory"); __builtin_amdgcn_sched_barrier(0); } while (0)
#define SPIN _Pragma("unroll 1") for (int spin = 0; spin < SPIN_CAP; ++spin)
__device__ __forceinline__ void wg_barrier() {
  __builtin_amdgcn_sched_barrier(0);
  asm volatile("s_waitcnt lgkmcnt(0)\n\ts_barrier" ::: "memory");
  __builtin_amdgcn_sched_barrier(0);
}
__device__ __forceinline__ rsrc_t mk(const void* p, u32 bytes) { return __builtin_amdgcn_make_buffer_rsrc((void*)p, 0, bytes, 0x00020000); }
__device__ __forceinline__ float asf(u32 x) { return __builtin_bit_cast(float, x); }
__device__ __forceinline__ u32 asu(float x) { return __builtin_bit_cast(u32, x); }
__device__ __forceinline__ u32 pk(float lo, float hi) { bf2 v = {(__bf16)lo, (__bf16)hi}; return __builtin_bit_cast(u32, v); }
__device__ __forceinline__ float rbf(float x) { return (float)(__bf16)x; }
__device__ __forceinline__ float lo16(u32 x) { return asf(x << 16); }
__device__ __forceinline__ float hi16(u32 x) { return asf(x & 0xffff0000u); }
template <int C> __device__ __forceinline__ float dpp(float x) { return asf(__builtin_amdgcn_mov_dpp(asu(x), C, 0xf, 0xf, true)); }
constexpr int XOR1 = 0xB1, XOR2 = 0x4E, HMIR = 0x141, ROR8 = 0x128;
__device__ __forceinline__ float sum16(float x) { auto s = __builtin_amdgcn_permlane16_swap(asu(x), asu(x), false, false); return asf(s[0]) + asf(s[1]); }
__device__ __forceinline__ float sum32(float x) { auto s = __builtin_amdgcn_permlane32_swap(asu(x), asu(x), false, false); return asf(s[0]) + asf(s[1]); }
__device__ __forceinline__ float max16(float x) { auto s = __builtin_amdgcn_permlane16_swap(asu(x), asu(x), false, false); return fmaxf(asf(s[0]), asf(s[1])); }
__device__ __forceinline__ float max32(float x) { auto s = __builtin_amdgcn_permlane32_swap(asu(x), asu(x), false, false); return fmaxf(asf(s[0]), asf(s[1])); }
__device__ __forceinline__ float sum8(float x) { x += dpp<XOR1>(x); x += dpp<XOR2>(x); x += dpp<HMIR>(x); return x; }
__device__ __forceinline__ float half_sum(float x) { x = sum8(x); x += dpp<ROR8>(x); return sum16(x); }
__device__ __forceinline__ float dot2(u32 a, u32 b, float c) { return __builtin_amdgcn_fdot2_f32_bf16(__builtin_bit_cast(bf2, a), __builtin_bit_cast(bf2, b), c, false); }
__device__ __forceinline__ v4f mfma32(v4u a, v4u b, v4f c) { return __builtin_amdgcn_mfma_f32_16x16x32_bf16(__builtin_bit_cast(bf8, a), __builtin_bit_cast(bf8, b), c, 0, 0, 0); }
__device__ __forceinline__ v4f mfma16(v2u a, v2u b, v4f c) { return __builtin_amdgcn_mfma_f32_16x16x16bf16_1k(__builtin_bit_cast(s4, a), __builtin_bit_cast(s4, b), c, 0, 0, 0); }
template <int N> __device__ __forceinline__ u32 dup(u32 x) { return __builtin_amdgcn_perm(x, x, (N & 1) ? 0x03020302u : 0x01000100u); }
__device__ __forceinline__ u32 nosent(float x) { u32 u = asu(x); return u < 0xFFFFFFFEu ? u : 0xFFFFFFFEu; }
__device__ __forceinline__ float ex2(float x) { return __builtin_amdgcn_exp2f(x); }
constexpr float LOG2E = 1.4426950408889634f;

__device__ __forceinline__ v4u enc4(v4f x, u32 T) {
  return v4u{__builtin_amdgcn_perm(asu(x[0]), T, 0x07060500u), __builtin_amdgcn_perm(asu(x[1]), T, 0x07060501u),
             __builtin_amdgcn_perm(asu(x[2]), T, 0x07060502u), __builtin_amdgcn_perm(asu(x[3]), T, 0x07060503u)};
}
__device__ __forceinline__ u32 tag4(v4u x) { return __builtin_amdgcn_perm(x[1], x[0], 0x0c0c0400u) | __builtin_amdgcn_perm(x[3], x[2], 0x04000c0cu); }
__device__ __forceinline__ float dec(u32 x) { return asf(x & 0xffffff00u); }
__device__ __forceinline__ v4u nibbles(u32 T, int odd) { const u32 t = T >> (16 * odd); return v4u{t & 15u, (t >> 4) & 15u, (t >> 8) & 15u, (t >> 12) & 15u}; }
__device__ __forceinline__ v4u enc4n(v4f x, u32 T, int odd) { return (__builtin_bit_cast(v4u, x) & 0xfffffff0u) | nibbles(T, odd); }
template <int STRIDE, int LANES> __device__ __forceinline__ v4f gather2(rsrc_t rW, u32 off, int l, u32 T) {
  v4u a, b;
  const v4u nb = nibbles(T, l & 1);
  SPIN {
    asm volatile("" ::: "memory");
    a = __builtin_amdgcn_raw_buffer_load_b128(rW, l < LANES ? off : OOB, 0, AUX_SC1);
    b = __builtin_amdgcn_raw_buffer_load_b128(rW, l < LANES ? off : OOB, STRIDE, AUX_SC1);
    const v4u m = (a ^ nb) | (b ^ nb);
    if (__all(l >= LANES || ((m[0] | m[1] | m[2] | m[3]) & 15u) == 0u)) break;
    __builtin_amdgcn_s_sleep(16);
  }
  return __builtin_bit_cast(v4f, a & 0xfffffff0u) + __builtin_bit_cast(v4f, b & 0xfffffff0u);
}
template <int SLEEP> __device__ __forceinline__ v4f final_sum(rsrc_t rW, u32 off, int base, u32 T) {
  v4u t[3];
  SPIN {
    asm volatile("" ::: "memory");
    sfor<0, 3>([&](auto I) { t[I] = __builtin_amdgcn_raw_buffer_load_b128(rW, off, base + I * 8 * 512, AUX_SC1); });
    if (__all(((tag4(t[0]) ^ T) | (tag4(t[1]) ^ T) | (tag4(t[2]) ^ T)) == 0u)) break;
    if (SLEEP) __builtin_amdgcn_s_sleep(SLEEP);
  }
  v4f s = __builtin_bit_cast(v4f, t[0] & 0xffffff00u) + __builtin_bit_cast(v4f, t[1] & 0xffffff00u) + __builtin_bit_cast(v4f, t[2] & 0xffffff00u);
  sfor<0, 4>([&](auto E) { s[E.value] = sum32(sum16(s[E.value] + dpp<ROR8>(s[E.value]))); });
  return s;
}
constexpr int win_of(int u) { return NPRE - u / 2 > WMIN ? NPRE - u / 2 : WMIN; }
constexpr int issued_before(int u) { return u + win_of(u) < 64 ? u + win_of(u) : 64; }
extern "C" __device__ u32 raw_load_b32(v4u, u32, u32, u32) __asm("llvm.amdgcn.raw.buffer.load.i32");
extern "C" __device__ u64 dispatch_id() __asm("llvm.amdgcn.dispatch.id");
__device__ __forceinline__ u32 clk() { return (u32)__builtin_amdgcn_s_memrealtime(); }
__device__ __forceinline__ u32 vm_out() { return __builtin_amdgcn_s_getreg(7 | (3 << 11)); }
__device__ __forceinline__ void pace(u32 lim) {
  u32 a;
  asm volatile("1:\n\ts_getreg_b32 %0, hwreg(HW_REG_IB_STS, 0, 4)\n\ts_memrealtime vcc\n\ts_max_u32 %0, %0, %2\n\ts_mulk_i32 %0, %3\n\ts_add_u32 %0, %0, %1\n\t"
               "s_waitcnt lgkmcnt(0)\n\ts_sub_u32 %0, vcc_lo, %0\n\ts_cmp_lt_i32 %0, 0\n\ts_cbranch_scc1 1b"
               : "=&s"(a) : "s"(lim), "n"(N0), "n"(CC) : "vcc", "scc", "memory");
}

__global__ void __launch_bounds__(512, 2)
fused_decode(const u16* x, const u16* kc, const u16* vc, const u16* wq, const u16* wo, const u16* wg, const u16* wu, const u16* wd,
             u16* out, unsigned char* ws) {
  __shared__ __attribute__((aligned(16))) unsigned char lds[L_END];
  u64 pc; u32 clen;
  asm volatile("s_getpc_b64 %0\n.Lcode_lo:\n\ts_mov_b32 %1, .Lcode_hi-.Lcode_lo" : "=s"(pc), "=s"(clen));
  const u32 cpf = raw_load_b32(v4u{(u32)pc, (u32)(pc >> 32) & 0xffffu, clen, 0x00020000u}, threadIdx.x * 2048 + blockIdx.x * 8, 0, 0);
  PIN();
  const int tid = threadIdx.x, l = tid & 63, w = __builtin_amdgcn_readfirstlane(tid >> 6);
  const int bid = blockIdx.x, xg = bid & 7, idx = bid >> 3;
  const int r = l >> 3, c = l & 7, g = l >> 4, col = l & 15;
  const bool pf = idx >= 26;
  const int ks = idx & 1, pu = idx >> 1, pcol = pu * 8 + xg - (pf ? 104 : 0);
  const u32 pstr = pf ? F_ * 2 : HD * 2;
  const u32 op = (u32)(r * 10 + 320 * ks + 80 * w) * pstr + (u32)(pcol * 128 + c * 16) + (pu == 12 ? OOB : 0u);
  const u32 oxl = (u32)(tid & 0x13f) * 40u;
  u32 vzero;
  asm("v_mov_b32 %0, 0" : "=v"(vzero));
  asm volatile("" :: "s"(x), "s"(kc), "s"(vc), "s"(wq), "s"(wo), "s"(wg), "s"(wu), "s"(wd), "s"(out), "s"(ws), "v"(op), "v"(oxl));
  const rsrc_t rX = mk(x, 2 * D_ * 2);
  const rsrc_t rW = mk(ws, WS_END);
  const v4u xa = __builtin_amdgcn_raw_buffer_load_b128(rX, oxl, 0, 0);
  const v4u xb = __builtin_amdgcn_raw_buffer_load_b128(rX, oxl + 16, 0, 0);
  const v2u xc = __builtin_amdgcn_raw_buffer_load_b64(rX, oxl + 32, 0, 0);
  const u32 e = __builtin_amdgcn_raw_buffer_load_b32(rW, vzero, WS_E, AUX_SC1);
  PIN();
  int h, b, sp, G, lc;
  bool tile;
  auto setup = [&]() {
    int i2 = idx;
    asm volatile("" : "+s"(i2));
    h = ((i2 >> 2) & 3) * 8 + xg; b = i2 >> 4; sp = i2 & 3;
    tile = i2 < 30; G = xg * 3 + i2 / 10; lc = i2 % 10;
  };
  auto tag_of = [&](u32 ep) { return ep + ((((u32)dispatch_id() * 0x9E3779B1u) & 0x7e7e7e7eu) | 0x01010101u); };
  auto wait_flag = [&](int off, auto SL) {
    SPIN {
      asm volatile("" ::: "memory");
      if (__builtin_amdgcn_readfirstlane(*(const u32*)(lds + off)) != SENT) break;
      __builtin_amdgcn_s_sleep(SL.value);
    }
    asm volatile("" ::: "memory");
  };

  if (w >= 4) {
    const int sw = w - 4;
    wg_barrier();
    setup();
    const int bh = b * H_ + h;
    const u32 okv = (u32)((sp * 32 + sw * 8 + r) * 384 + c * 16);
    auto kvstep = [&](int i) { return (u32)(((i + bh * ROT) & 31) * 49152); };
    const rsrc_t rK = mk((const unsigned char*)kc + (u32)(bh * (SK * DH * 2)), SK * DH * 2);
    const rsrc_t rV = mk((const unsigned char*)vc + (u32)(bh * (SK * DH * 2)), SK * DH * 2);
    v4u KV[64][3];
    u32 zt[3];
    {
      const u32 o3 = (u32)(l * 524288 - (l == 3 ? 4 : 0)) | (l < 4 ? 0u : OOB);
      zt[0] = __builtin_amdgcn_raw_buffer_load_b32(rK, o3 | (sw == 0 ? 0u : OOB), 0, AUX_NT);
      zt[1] = __builtin_amdgcn_raw_buffer_load_b32(rV, o3 | (sw == 1 ? 0u : OOB), 0, AUX_NT);
      zt[2] = __builtin_amdgcn_raw_buffer_load_b32(mk(wo, HD * D_ * 2), (u32)((256 * G + 255 * l) * (D_ * 2)) | ((l < 2 && sw == 2) ? 0u : OOB), 0, AUX_NT);
      PIN();
    }
    const int i0 = ((l + 1 - b) & ~1) + b - 1, i1 = i0 < 63 ? i0 + 1 : 63;
    const int tp = TOFF + (((i0 + i1) * 8 + ((bh >> 1) & 15) - 8) * TAU >> 8);
    const int tl = tp - N0 * CC, tm = tp + (C0 + 3) * (TAU / 48), otb = (i0 > 0 ? i0 : 0) * 4;
    u32 tb = *(const u32*)(lds + L_TB + l * 4);
    const u32 orep = l == 0 ? (u32)((b * 8 + xg) * PSTR) : OOB;
    const int rep = sw == 0 && idx == 5 ? 0 : sw == 0 && idx == REP1 ? 1 : -1;
    auto iss = [&](auto I) {
      pace(__builtin_amdgcn_readlane(tb + tl, I));
      constexpr int J = (I + 1) & ~1;
      if constexpr (J >= CK0 && (J - CK0) % CKS == 0 && (J - CK0) / CKS < NCK) {
        if (rep == (I & 1)) {
          const u32 now = clk(), v = now + vm_out() * (TAU / 48) - __builtin_amdgcn_readlane(tm, I);
          if ((int)(now - __builtin_amdgcn_readlane(tb, I)) < TOFF + (J + CKD - RLEAD) * TAU / 16 - LATE)
            __builtin_amdgcn_raw_buffer_store_b64(v2u{v, tag_of(e)}, rW, orep, WS_P + (J - CK0) / CKS * 16 * PSTR, AUX_SC1);
        }
      }
      sfor<0, 3>([&](auto P) { KV[I][P] = __builtin_amdgcn_raw_buffer_load_b128(I < 32 ? rK : rV, okv + P * 128, kvstep(I), AUX_NT); });
      if constexpr (I >= CK0 + CKD - 2) tb = *(const u32*)(lds + L_TB + otb);
      PIN();
    };
    sfor<0, NPRE>(iss);
    asm volatile("" :: "v"(zt[0]), "v"(zt[1]), "v"(zt[2]), "v"(cpf), "v"(e));
    if (sw == 0) STAMP(11);
    wait_flag(L_FQ, IC(1));
    v4u q[3];
    sfor<0, 3>([&](auto P) { q[P] = *(const v4u*)(lds + L_Q + P * 128 + c * 16); });
    PIN();
    float sc[32], mw;
    sfor<0, 32>([&](auto I) {
      float d0 = 0.f, d1 = 0.f, d2 = 0.f;
      sfor<0, 4>([&](auto E) { d0 = dot2(KV[I][0][E.value], q[0][E.value], d0); d1 = dot2(KV[I][1][E.value], q[1][E.value], d1); d2 = dot2(KV[I][2][E.value], q[2][E.value], d2); });
      sc[I] = rbf(rbf(sum8(d0 + d1 + d2)) * 0.07216878364870322f);
      mw = I == 0 ? sc[I] : fmaxf(mw, sc[I]);
      asm volatile("" : "+v"(sc[I]), "+v"(mw));
      PIN();
      sfor<issued_before(I), issued_before(I + 1)>(iss);
    });
    mw = fmaxf(mw, dpp<ROR8>(mw)); mw = max32(max16(mw));
    v4u msk;
    sfor<0, 4>([&](auto I) { msk[I.value] = (c >> 1) == I ? ((c & 1) ? 0xffff0000u : 0x0000ffffu) : 0u; });
    float lw = 0.f;
    u32 pp[32];
    v4f acc[3] = {{0, 0, 0, 0}, {0, 0, 0, 0}, {0, 0, 0, 0}};
    sfor<32, 64>([&](auto I) {
      if constexpr (I < 40) {
        sfor<(I - 32) * 4, (I - 31) * 4>([&](auto N) { const float p = ex2((sc[N] - mw) * LOG2E); lw += p; pp[N] = pk(p, p); asm volatile("" : "+v"(pp[N])); });
        asm volatile("" : "+v"(lw));
      }
      if constexpr (I == 39) {
        lw += dpp<ROR8>(lw); lw = sum32(sum16(lw));
        *(v2u*)(lds + L_ML + sw * 8) = v2u{nosent(mw), nosent(lw)};
      }
      const v4u a = msk & pp[I - 32];
      sfor<0, 3>([&](auto P) { acc[P] = mfma32(a, KV[I][P], acc[P]); });
      if constexpr (I < 63) asm volatile("" : "+v"(acc[0]), "+v"(acc[1]), "+v"(acc[2]));
      PIN();
      sfor<issued_before(I), issued_before(I + 1)>(iss);
      if constexpr (I == GSTEP) { *(u32*)(lds + L_FP) = 1u; PIN(); }
      if constexpr (I == 63 - WMIN) { *(u32*)(lds + L_FW) = 1u; PIN(); }
    });
    if ((g >> 1) == (col >> 3)) sfor<0, 3>([&](auto P) { *(v4f*)(lds + L_RO + ((2 * sw + (g >> 1)) * 192 + c * 8 + 4 * (g & 1)) * 4 + P * 256) = acc[P]; });
    wg_barrier();
    asm volatile(".Lcode_hi:");
    return;
  }

  v4u P0[10], P1[10], WO[8], F2[8];
  {
    const rsrc_t rP = mk(pf ? wg : wq, D_ * pstr);
    sfor<0, 10>([&](auto I) { P0[I] = __builtin_amdgcn_raw_buffer_load_b128(rP, op, I * pstr, AUX_NT); __builtin_amdgcn_sched_barrier(0); });
    PIN();
  }
  setup();
  if (w == 0) {
    if (l < 3) *(v4u*)(lds + L_ML + l * 16) = v4u{SENT, SENT, SENT, SENT};
  }
  if (w == 1) { asm volatile("" :: "v"(e)); PIN(); *(u32*)(lds + L_TB + l * 4) = clk(); STAMP(12); }
  wg_barrier();
  auto issWO = [&](auto LO, auto HI) {
    const u32 o = (u32)((256 * G + 64 * w + 4 * r) * (D_ * 2) + lc * 128 + c * 16) | (tile ? 0u : OOB);
    sfor<LO, HI>([&](auto N) { WO[N] = __builtin_amdgcn_raw_buffer_load_b128(mk(wo, HD * D_ * 2), o, (N / 4 * 32 + N % 4) * (D_ * 2), AUX_NT); });
    PIN();
  };
  auto issU = [&]() {
    if (pf) sfor<0, 10>([&](auto I) { P1[I] = __builtin_amdgcn_raw_buffer_load_b128(mk(wu, D_ * F_ * 2), op, I * (F_ * 2), AUX_NT); });
    PIN();
  };
  issWO(IC(0), IC(WE));
  if (UPOS == 0) issU();
  const u32 T = tag_of(e);
  {
    const u32 xd[10] = {xa[0], xa[1], xa[2], xa[3], xb[0], xb[1], xb[2], xb[3], xc[0], xc[1]};
    float s = 0.f;
    sfor<0, 10>([&](auto I) { s += lo16(xd[I]) + hi16(xd[I]); });
    const float mean = half_sum(s) * (1.f / D_);
    float v = 0.f;
    sfor<0, 10>([&](auto I) { const float d0 = lo16(xd[I]) - mean, d1 = hi16(xd[I]) - mean; v += d0 * d0 + d1 * d1; });
    const float var = half_sum(v) * (1.f / D_);
    const float mb = rbf(mean), inv = __builtin_amdgcn_rcpf(rbf(__builtin_sqrtf(rbf(rbf(var) + 1e-5f))));
    sfor<0, 5>([&](auto I) {
      const u32 d0 = pk(lo16(xd[2 * I]) - mb, hi16(xd[2 * I]) - mb), d1 = pk(lo16(xd[2 * I + 1]) - mb, hi16(xd[2 * I + 1]) - mb);
      *(v2u*)(lds + L_XN + w * 2560 + l * 40 + I * 8) = v2u{pk(lo16(d0) * inv, hi16(d0) * inv), pk(lo16(d1) * inv, hi16(d1) * inv)};
    });
    PIN();
  }
  if (UPOS == 1) issU();
  sfor<0, 8>([&](auto I) { F2[I] = __builtin_amdgcn_raw_buffer_load_b128(mk(wd, F_ * D_ * 2), (u32)((64 * G + 8 * r) * (D_ * 2) + lc * 128 + c * 16) | ((w == 2 && tile) ? 0u : OOB), I * (D_ * 2), AUX_NT); });
  PIN();
  const u32 t0 = __builtin_amdgcn_readfirstlane(*(const u32*)(lds + L_TB));
  auto wait_clock = [&](int ticks) { while ((int)(clk() - t0) < ticks) __builtin_amdgcn_s_sleep(2); };
  const int ab = col >> 3, ar = 2 * g + ((col >> 2) & 1);
  const u32 hm = (l & 1) ? 0xffff0000u : 0x0000ffffu;
  u32 m0 = (l & 2) ? 0u : hm, m1 = (l & 2) ? hm : 0u;
  auto gemv = [&](u32 d, v4u W, v4f& lo, v4f& hi) { const v2u A = {d & m0, d & m1}; lo = mfma16(A, v2u{W[0], W[1]}, lo); hi = mfma16(A, v2u{W[2], W[3]}, hi); };
  const bool dv = (g & 1) == (col >> 3);
  const u32 ld0 = (u32)((g & 1) * 512 + (g >> 1) * 256 + c * 32);
  auto put = [&](int slot, v4f lo, v4f hi) { if (dv) { *(v4f*)(lds + L_RS + slot * 1024 + ld0) = lo; *(v4f*)(lds + L_RS + slot * 1024 + ld0 + 16) = hi; } };
  auto sum4 = [&](int slot) {
    v4f t[4];
    sfor<0, 4>([&](auto I) { t[I] = *(const v4f*)(lds + L_RS + (slot + I) * 1024 + l * 16); });
    v4f s = (t[0] + t[1]) + (t[2] + t[3]);
    sfor<0, 4>([&](auto E) { s[E.value] = sum32(s[E.value]); });
    return s;
  };
  {
    u32 xv[5];
    sfor<0, 5>([&](auto I) { xv[I] = *(const u32*)(lds + L_XN + w * 2560 + ab * 1280 + (320 * ks + 80 * w + 10 * ar) * 2 + I * 4); });
    v4f lo = {0, 0, 0, 0}, hi = {0, 0, 0, 0};
    sfor<0, 10>([&](auto I) { gemv(dup<I>(xv[I / 2]), P0[I], lo, hi); });
    put(w, lo, hi);
    if (pf) {
      lo = v4f{0, 0, 0, 0}; hi = v4f{0, 0, 0, 0};
      sfor<0, 10>([&](auto I) { gemv(dup<I>(xv[I / 2]), P1[I], lo, hi); });
      put(4 + w, lo, hi);
    }
    asm volatile("" ::: "memory");
    u32 n = 0;
    if (l == 0) n = __hip_atomic_fetch_add((u32*)(lds + L_CNT), 1u, __ATOMIC_RELAXED, __HIP_MEMORY_SCOPE_WORKGROUP);
    if (__builtin_amdgcn_readfirstlane(n) == SENT + 3u) {
      asm volatile("" ::: "memory");
      const u32 o = pf ? (u32)(WS_F + (pcol * 2 + ks) * 1024 + l * 16)
                       : (u32)(WS_Q + ks * 49152 + (l >> 4) * 24576 + pcol * 256 + (l & 15) * 16);
      const u32 om = o | ((l < 32 && pu != 12) ? 0u : OOB);
      __builtin_amdgcn_raw_buffer_store_b128(enc4n(sum4(0), T, l & 1), rW, om, 0, AUX_SC1);
      if (pf) __builtin_amdgcn_raw_buffer_store_b128(enc4n(sum4(4), T, l & 1), rW, om, 512, AUX_SC1);
      STAMP(13);
    }
    PIN();
  }
  const int me = h * 8 + b * 4 + sp;
  const bool fin = w == 0 && bid < 40;
  const int fq = (bid & 3) * 8 + c;
  const u32 ofin = (u32)(((bid >> 2) * 24 + r) * 512 + fq * 16);
  const int oidx = (fq >> 4) * D_ + (bid >> 2) * 64 + (fq & 15) * 4;
  v4f gate = {0, 0, 0, 0}, fw = {0, 0, 0, 0};
  v2u x4 = {0, 0};

  if (w == 3) {
    wait_clock(QT);
    const v4f s = gather2<49152, 48>(rW, (u32)(WS_Q + b * 24576 + h * 768 + l * 16), l, T);
    if (l < 48) *(v2u*)(lds + L_Q + l * 8) = v2u{pk(s[0], s[1]), pk(s[2], s[3])};
    asm volatile("" ::: "memory");
    if (l == 0) *(u32*)(lds + L_FQ) = 1u;
    STAMP(3);
  } else if (w == 2) {
    if (tile) {
      wait_clock(FT);
      const v4f s = gather2<1024, 64>(rW, (u32)(WS_F + G * 2048 + l * 16), l, T);
      float hv[4];
      sfor<0, 4>([&](auto E) {
        const auto t = __builtin_amdgcn_permlane32_swap(asu(s[E.value]), asu(s[E.value]), false, false);
        const float gg = rbf(asf(t[0])), uu = rbf(asf(t[1]));
        const float z = 0.7978845608028654f * (gg + 0.044715f * gg * gg * gg);
        hv[E] = rbf(gg / (1.f + ex2(-2.f * LOG2E * z))) * uu;
      });
      if (l < 32) *(v2u*)(lds + L_H + l * 8) = v2u{pk(hv[0], hv[1]), pk(hv[2], hv[3])};
      const v4u hh = *(const v4u*)(lds + L_H + ab * 128 + ar * 16);
      v4f lo = {0, 0, 0, 0}, hi = {0, 0, 0, 0};
      sfor<0, 8>([&](auto I) { gemv(dup<I>(hh[I / 2]), F2[I], lo, hi); });
      put(8, lo, hi);
      v4f y = *(const v4f*)(lds + L_RS + 8 * 1024 + l * 16);
      sfor<0, 4>([&](auto E) { y[E.value] = sum32(y[E.value]); });
      __builtin_amdgcn_raw_buffer_store_b128(enc4(y, T), rW, l < 32 ? (u32)(WS_Y + (lc * 24 + G) * 512 + l * 16) : OOB, 0, AUX_SC1);
    }
  } else if (w == 1) {
    int base = 0, m1_ = 0, m2_ = 0;
    bool tilted = false;
    _Pragma("unroll 1") for (int k = 0; k < NCK; ++k) {
      const int S = CK0 + k * CKS, E = S + CKD, end = S < 32 ? 32 : 64, bS = __builtin_amdgcn_readlane(base, S);
      wait_clock(__builtin_amdgcn_readlane(base, E - RLEAD) + TOFF + (E - RLEAD) * TAU / 16);
      asm volatile("" ::: "memory");
      const v2u z = __builtin_amdgcn_raw_buffer_load_b64(rW, (u32)(WS_P + (k * 16 + col) * PSTR), 0, PAUX);
      int v = (int)(z[0] - t0);
      if (z[1] != T || (u32)(v + 1000) > 2000u) v = bS + MISS;
      v += __builtin_amdgcn_mov_dpp(v, XOR1, 0xf, 0xf, true); v += __builtin_amdgcn_mov_dpp(v, XOR2, 0xf, 0xf, true);
      v += __builtin_amdgcn_mov_dpp(v, HMIR, 0xf, 0xf, true); v += __builtin_amdgcn_mov_dpp(v, ROR8, 0xf, 0xf, true);
      const int m = __builtin_amdgcn_readfirstlane(v) >> 4;
      if (S == 32) tilted = false;
      if (k == 0) base = l >= E ? m + MARG0 - (l > 32 ? (l - 32) * XV / 16 : 0) : 0;
      else {
        const int bl = m - __builtin_amdgcn_readlane(base, E + 3 < 63 ? E + 3 : 63);
        int d = 0;
        if (k == 1) d = bl - KEEP1;
        else if (!tilted && S >= end - (S < 32 ? 14 : 24) && m - m2_ >= (S < 32 ? GK : GV) && m - bS >= LV) {
          d = bl + 3 * (CKD < end - S ? CKD : end - S) - REST;
          const int n2 = (l < end ? l : end) - E;
          base += n2 > 0 ? n2 * (2 * SIG + (S < 32 ? 0 : XV)) / 16 : 0;
          tilted = true;
        } else if (bl > LMAX - (S > 32 ? (S - 32) * USL / 16 : 0)) d = bl - KEEP;
        const int rp = RAMP * (l - (E - 1));
        if (d > 0) base += rp < 0 ? 0 : rp < d ? rp : d;
      }
      m2_ = m1_; m1_ = m;
      *(u32*)(lds + L_TB + l * 4) = t0 + base;
    }
  } else {
    wait_flag(L_FQ, IC(16));
    x4 = __builtin_amdgcn_raw_buffer_load_b64(rX, fin ? (u32)(oidx * 2) : OOB, 0, 0);
    {
      const u32 z = __builtin_amdgcn_raw_buffer_load_b32(rW, l < 6 ? (u32)(me * REC + R_O + l * 128) : (l < 10 && tile) ? (u32)(WS_S + (lc * 24 + G) * 512 + (l - 6) * 128) : OOB, 0, AUX_SC1);
      asm volatile("" :: "v"(z));
    }
    v2u mlw;
    SPIN {
      asm volatile("" ::: "memory");
      mlw = *(const v2u*)(lds + L_ML + (l & 3) * 8);
      if (__all(mlw[0] != SENT && mlw[1] != SENT)) break;
      __builtin_amdgcn_s_sleep(16);
    }
    STAMP(5);
    float Mg = asf(mlw[0]); Mg = fmaxf(Mg, dpp<XOR1>(Mg)); Mg = fmaxf(Mg, dpp<XOR2>(Mg));
    float Lg = asf(mlw[1]) * ex2((asf(mlw[0]) - Mg) * LOG2E); Lg += dpp<XOR1>(Lg); Lg += dpp<XOR2>(Lg);
    __builtin_amdgcn_raw_buffer_store_b128(enc4(v4f{Mg, Lg, 0.f, 0.f}, T), rW, l < 8 ? (u32)(me * REC + R_ML + l * 16) : OOB, 0, AUX_SC1);
    if (fin) {
      const v4f y4 = final_sum<16>(rW, ofin, WS_Y, T);
      sfor<0, 4>([&](auto E) { const float y = rbf(y4[E.value]); gate[E.value] = rbf(y / (1.f + ex2(-LOG2E * y))); });
    }
    wait_flag(L_FP, IC(16));
    v4u ml4;
    SPIN {
      asm volatile("" ::: "memory");
      ml4 = __builtin_amdgcn_raw_buffer_load_b128(rW, (u32)((h * 8 + b * 4 + (l & 3)) * REC + R_ML + ((l >> 2) & 7) * 16), 0, AUX_SC1);
      if (__all(tag4(ml4) == T)) break;
      __builtin_amdgcn_s_sleep(16);
    }
    float M = dec(ml4[0]); M = fmaxf(M, dpp<XOR1>(M)); M = fmaxf(M, dpp<XOR2>(M));
    float L = dec(ml4[1]) * ex2((dec(ml4[0]) - M) * LOG2E); L += dpp<XOR1>(L); L += dpp<XOR2>(L);
    const float f = ex2((asf(mlw[0]) - M) * LOG2E) / L;
    fw = v4f{dpp<0x00>(f), dpp<0x55>(f), dpp<0xAA>(f), dpp<0xFF>(f)};
  }

  asm volatile("" :: "v"(cpf));
  __builtin_amdgcn_s_waitcnt(0x0F70);
  const int R0 = 256 * G + 64 * w, hh = R0 / DH;
  u32 oo = (u32)((hh * 8 + ab * 4 + (l & 3)) * REC + R_O + (R0 - hh * DH + 4 * ar) * 4);
  u32 ost = l < 48 ? (u32)(me * REC + R_O + l * 16) : OOB, oss = l < 32 ? (u32)(WS_S + (lc * 24 + G) * 512 + l * 16) : OOB;
  u32 oout = l < 8 ? (u32)(oidx * 2) : OOB, oep = (bid == 0 && l == 0) ? (u32)WS_E : OOB;
  u32 lrs = dv ? (u32)(L_RS + w * 1024) + ld0 : (u32)(L_SP + l * 32);
  v4f xf = {lo16(x4[0]), hi16(x4[0]), lo16(x4[1]), hi16(x4[1])};
  asm volatile("" : "+v"(oo), "+v"(m0), "+v"(m1), "+v"(ost), "+v"(oss), "+v"(oout), "+v"(oep), "+v"(lrs), "+v"(xf), "+v"(fw), "+v"(gate));
  wait_flag(L_FW, IC(2));
  {
    const u32 tw = __builtin_amdgcn_readfirstlane(*(const u32*)(lds + L_TB + 63 * 4)) + (TOFF + 63 * TAU / 16 + WOT);
    while ((int)(clk() - tw) < 0) __builtin_amdgcn_s_sleep(1);
  }
  issWO(IC(WE), IC(8));
  wg_barrier();
  if (w == 0) {
    STAMP(7);
    v4f t[8];
    sfor<0, 8>([&](auto I) { t[I] = *(const v4f*)(lds + L_RO + I * 768 + l * 16); });
    __builtin_amdgcn_sched_barrier(0);
    const v4f s = ((t[0] + t[1]) * fw[0] + (t[2] + t[3]) * fw[1]) + ((t[4] + t[5]) * fw[2] + (t[6] + t[7]) * fw[3]);
    __builtin_amdgcn_raw_buffer_store_b128(enc4(s, T), rW, ost, 0, AUX_SC1);
  } else {
    __builtin_amdgcn_s_sleep(S1 - S0);
  }
  if (tile) {
    __builtin_amdgcn_s_sleep(S0);
    v4u o4[2];
    SPIN {
      asm volatile("" ::: "memory");
      o4[0] = __builtin_amdgcn_raw_buffer_load_b128(rW, oo, 0, AUX_SC1);
      o4[1] = __builtin_amdgcn_raw_buffer_load_b128(rW, oo + 128, 0, AUX_SC1);
      if (__all(((tag4(o4[0]) ^ T) | (tag4(o4[1]) ^ T)) == 0u)) break;
    }
    if (w == 0) STAMP(8);
    float v[8];
    sfor<0, 8>([&](auto E) { v[E] = dec(o4[E / 4][E % 4]); });
    __builtin_amdgcn_sched_barrier(0);
    sfor<0, 8>([&](auto E) { v[E] += dpp<XOR1>(v[E]); });
    __builtin_amdgcn_sched_barrier(0);
    sfor<0, 8>([&](auto E) { v[E] += dpp<XOR2>(v[E]); });
    __builtin_amdgcn_sched_barrier(0);
    v2u A[8];
    auto build = [&](auto E) { const u32 d = pk(v[E], v[E]); A[E] = v2u{d & m0, d & m1}; };
    build(IC(0));
    v4f lo = {0, 0, 0, 0}, hi = {0, 0, 0, 0};
    sfor<0, 8>([&](auto E) {
      if constexpr (E < 7) build(IC(E + 1));
      __builtin_amdgcn_sched_barrier(0);
      lo = mfma16(A[E], v2u{WO[E][0], WO[E][1]}, lo); hi = mfma16(A[E], v2u{WO[E][2], WO[E][3]}, hi);
      __builtin_amdgcn_sched_barrier(0);
    });
    *(v4f*)(lds + lrs) = lo; *(v4f*)(lds + lrs + 16) = hi;
    wg_barrier();
    if (w == 0) {
      __builtin_amdgcn_raw_buffer_store_b128(enc4(sum4(0), T), rW, oss, 0, AUX_SC1);
      if (fin) {
        __builtin_amdgcn_s_sleep(S2);
        const v4f s4 = final_sum<0>(rW, ofin, WS_S, T);
        float o[4];
        sfor<0, 4>([&](auto E) { o[E] = xf[E.value] + rbf(rbf(s4[E.value]) * gate[E.value]); });
        __builtin_amdgcn_raw_buffer_store_b64(v2u{pk(o[0], o[1]), pk(o[2], o[3])}, mk(out, 2 * D_ * 2), oout, 0, AUX_SC1);
        __builtin_amdgcn_raw_buffer_store_b32(T, rW, oep, 0, AUX_SC1);
      }
    }
  }
}

extern "C" void kernel_launch(void* const* d_in, const int* in_sizes, int n_in, void* d_out, int out_size, void* d_ws, size_t ws_size,
                              hipStream_t stream) {
  fused_decode<<<256, 512, 0, stream>>>((const u16*)d_in[0], (const u16*)d_in[1], (const u16*)d_in[2], (const u16*)d_in[3], (const u16*)d_in[4],
                                        (const u16*)d_in[6], (const u16*)d_in[7], (const u16*)d_in[8], (u16*)d_out, (unsigned char*)d_ws);
}
```

```cpp
#include <hip/hip_runtime.h>
#include <stdint.h>
#include <type_traits>

typedef unsigned u32;
typedef unsigned long long u64;
typedef unsigned short u16;
typedef u32 v4u __attribute__((ext_vector_type(4)));
typedef u32 v2u __attribute__((ext_vector_type(2)));
typedef float v4f __attribute__((ext_vector_type(4)));
typedef short s4 __attribute__((ext_vector_type(4)));
typedef __bf16 bf8 __attribute__((ext_vector_type(8)));
typedef __bf16 bf2 __attribute__((ext_vector_type(2)));
typedef __amdgpu_buffer_rsrc_t rsrc_t;

constexpr int D_ = 640, H_ = 32, SK = 4096, DH = 192, F_ = 1536, HD = H_ * DH;
constexpr int AUX_NT = 2, AUX_SC1 = 16;
constexpr u32 OOB = 0x80000000u;
constexpr u32 SENT = 0xFFFFFFFFu;
constexpr int SPIN_CAP = 1 << 18;

constexpr int WS_E = 0;
constexpr int REC = 896, R_O = 256, R_ML = R_O + 768;
constexpr int WS_Q = R_O + 256 * REC;
constexpr int WS_F = WS_Q + 64 * 1536;
constexpr int WS_Y = WS_F + 48 * 1024;
constexpr int WS_S = WS_Y + 240 * 512;
constexpr int WS_P = WS_S + 240 * 512, PSTR = 32;
constexpr int WS_END = WS_P + 16 * 16 * PSTR;

constexpr int NPRE = 14, WMIN = 6;
constexpr int QT = 410, FT = 820;
constexpr int GSTEP = 49;
constexpr int TAU = 665, XV = 14;
constexpr int TOFF = 190;
constexpr int N0 = 6, CC = 40;
constexpr int ROT = 7;
constexpr int WE = 4, WOT = 55;
constexpr int S0 = 8, S1 = 12, S2 = 12;
constexpr int CK0 = 4, CKS = 4, NCK = 14;
constexpr int CKD = 7, RLEAD = 3;
constexpr int C0 = 2, LATE = 90, MISS = 130;
constexpr int KEEP1 = 28, LMAX = 46, USL = 8, KEEP = 18;
constexpr int GK12 = 68, GK16 = 40, GK = 30, GV = 14, LV = 26, GROW = 3, REST = 5, SIG = 23, RAMP = 16;

#define IC(n) std::integral_constant<int, (n)>{}

constexpr int L_XN = 0;
constexpr int L_Q = L_XN + 4 * 2560;
constexpr int L_H = L_Q + 384;
constexpr int L_ML = L_H + 256;
constexpr int L_FQ = L_ML + 32, L_FP = L_ML + 36, L_CNT = L_ML + 40, L_FW = L_ML + 44;
constexpr int L_TB = L_ML + 64;
constexpr int L_RO = L_TB + 256;
constexpr int L_RS = L_RO + 8 * 768;
constexpr int L_SP = L_RS + 9 * 1024;
constexpr int L_END = L_SP + 2048;

template <int I, int N, class F> __device__ __forceinline__ void sfor(F&& f) {
  if constexpr (I < N) { f(std::integral_constant<int, I>{}); sfor<I + 1, N>(f); }
}
#define PIN() do { asm volatile("" ::: "memory"); __builtin_amdgcn_sched_barrier(0); } while (0)
#define SPIN _Pragma("unroll 1") for (int spin = 0; spin < SPIN_CAP; ++spin)
__device__ __forceinline__ void wg_barrier() {
  __builtin_amdgcn_sched_barrier(0);
  asm volatile("s_waitcnt lgkmcnt(0)\n\ts_barrier" ::: "memory");
  __builtin_amdgcn_sched_barrier(0);
}
__device__ __forceinline__ rsrc_t mk(const void* p, u32 bytes) { return __builtin_amdgcn_make_buffer_rsrc((void*)p, 0, bytes, 0x00020000); }
__device__ __forceinline__ float asf(u32 x) { return __builtin_bit_cast(float, x); }
__device__ __forceinline__ u32 asu(float x) { return __builtin_bit_cast(u32, x); }
__device__ __forceinline__ u32 pk(float lo, float hi) { bf2 v = {(__bf16)lo, (__bf16)hi}; return __builtin_bit_cast(u32, v); }
__device__ __forceinline__ float rbf(float x) { return (float)(__bf16)x; }
__device__ __forceinline__ float lo16(u32 x) { return asf(x << 16); }
__device__ __forceinline__ float hi16(u32 x) { return asf(x & 0xffff0000u); }
template <int C> __device__ __forceinline__ float dpp(float x) { return asf(__builtin_amdgcn_mov_dpp(asu(x), C, 0xf, 0xf, true)); }
constexpr int XOR1 = 0xB1, XOR2 = 0x4E, HMIR = 0x141, ROR8 = 0x128;
__device__ __forceinline__ float sum16(float x) { auto s = __builtin_amdgcn_permlane16_swap(asu(x), asu(x), false, false); return asf(s[0]) + asf(s[1]); }
__device__ __forceinline__ float sum32(float x) { auto s = __builtin_amdgcn_permlane32_swap(asu(x), asu(x), false, false); return asf(s[0]) + asf(s[1]); }
__device__ __forceinline__ float max16(float x) { auto s = __builtin_amdgcn_permlane16_swap(asu(x), asu(x), false, false); return fmaxf(asf(s[0]), asf(s[1])); }
__device__ __forceinline__ float max32(float x) { auto s = __builtin_amdgcn_permlane32_swap(asu(x), asu(x), false, false); return fmaxf(asf(s[0]), asf(s[1])); }
__device__ __forceinline__ float sum8(float x) { x += dpp<XOR1>(x); x += dpp<XOR2>(x); x += dpp<HMIR>(x); return x; }
__device__ __forceinline__ float half_sum(float x) { x = sum8(x); x += dpp<ROR8>(x); return sum16(x); }
__device__ __forceinline__ float dot2(u32 a, u32 b, float c) { return __builtin_amdgcn_fdot2_f32_bf16(__builtin_bit_cast(bf2, a), __builtin_bit_cast(bf2, b), c, false); }
__device__ __forceinline__ v4f mfma32(v4u a, v4u b, v4f c) { return __builtin_amdgcn_mfma_f32_16x16x32_bf16(__builtin_bit_cast(bf8, a), __builtin_bit_cast(bf8, b), c, 0, 0, 0); }
__device__ __forceinline__ v4f mfma16(v2u a, v2u b, v4f c) { return __builtin_amdgcn_mfma_f32_16x16x16bf16_1k(__builtin_bit_cast(s4, a), __builtin_bit_cast(s4, b), c, 0, 0, 0); }
template <int N> __device__ __forceinline__ u32 dup(u32 x) { return __builtin_amdgcn_perm(x, x, (N & 1) ? 0x03020302u : 0x01000100u); }
__device__ __forceinline__ u32 nosent(float x) { u32 u = asu(x); return u < 0xFFFFFFFEu ? u : 0xFFFFFFFEu; }
__device__ __forceinline__ float ex2(float x) { return __builtin_amdgcn_exp2f(x); }
constexpr float LOG2E = 1.4426950408889634f;

__device__ __forceinline__ v4u enc4(v4f x, u32 T) {
  return v4u{__builtin_amdgcn_perm(asu(x[0]), T, 0x07060500u), __builtin_amdgcn_perm(asu(x[1]), T, 0x07060501u),
             __builtin_amdgcn_perm(asu(x[2]), T, 0x07060502u), __builtin_amdgcn_perm(asu(x[3]), T, 0x07060503u)};
}
__device__ __forceinline__ u32 tag4(v4u x) { return __builtin_amdgcn_perm(x[1], x[0], 0x0c0c0400u) | __builtin_amdgcn_perm(x[3], x[2], 0x04000c0cu); }
__device__ __forceinline__ float dec(u32 x) { return asf(x & 0xffffff00u); }
__device__ __forceinline__ v4u nibbles(u32 T, int odd) { const u32 t = T >> (16 * odd); return v4u{t & 15u, (t >> 4) & 15u, (t >> 8) & 15u, (t >> 12) & 15u}; }
__device__ __forceinline__ v4u enc4n(v4f x, u32 T, int odd) { return (__builtin_bit_cast(v4u, x) & 0xfffffff0u) | nibbles(T, odd); }
template <int STRIDE, int LANES> __device__ __forceinline__ v4f gather2(rsrc_t rW, u32 off, int l, u32 T) {
  v4u a, b;
  const v4u nb = nibbles(T, l & 1);
  SPIN {
    asm volatile("" ::: "memory");
    a = __builtin_amdgcn_raw_buffer_load_b128(rW, l < LANES ? off : OOB, 0, AUX_SC1);
    b = __builtin_amdgcn_raw_buffer_load_b128(rW, l < LANES ? off : OOB, STRIDE, AUX_SC1);
    const v4u m = (a ^ nb) | (b ^ nb);
    if (__all(l >= LANES || ((m[0] | m[1] | m[2] | m[3]) & 15u) == 0u)) break;
    __builtin_amdgcn_s_sleep(16);
  }
  return __builtin_bit_cast(v4f, a & 0xfffffff0u) + __builtin_bit_cast(v4f, b & 0xfffffff0u);
}
template <int SLEEP> __device__ __forceinline__ v4f final_sum(rsrc_t rW, u32 off, int base, u32 T) {
  v4u t[3];
  SPIN {
    asm volatile("" ::: "memory");
    sfor<0, 3>([&](auto I) { t[I] = __builtin_amdgcn_raw_buffer_load_b128(rW, off, base + I * 8 * 512, AUX_SC1); });
    if (__all(((tag4(t[0]) ^ T) | (tag4(t[1]) ^ T) | (tag4(t[2]) ^ T)) == 0u)) break;
    if (SLEEP) __builtin_amdgcn_s_sleep(SLEEP);
  }
  v4f s = __builtin_bit_cast(v4f, t[0] & 0xffffff00u) + __builtin_bit_cast(v4f, t[1] & 0xffffff00u) + __builtin_bit_cast(v4f, t[2] & 0xffffff00u);
  sfor<0, 4>([&](auto E) { s[E.value] = sum32(sum16(s[E.value] + dpp<ROR8>(s[E.value]))); });
  return s;
}
constexpr int win_of(int u) { return NPRE - u / 2 > WMIN ? NPRE - u / 2 : WMIN; }
constexpr int issued_before(int u) { return u + win_of(u) < 64 ? u + win_of(u) : 64; }
extern "C" __device__ u32 raw_load_b32(v4u, u32, u32, u32) __asm("llvm.amdgcn.raw.buffer.load.i32");
extern "C" __device__ u64 dispatch_id() __asm("llvm.amdgcn.dispatch.id");
__device__ __forceinline__ u32 clk() { return (u32)__builtin_amdgcn_s_memrealtime(); }
__device__ __forceinline__ u32 vm_out() { return __builtin_amdgcn_s_getreg(7 | (3 << 11)); }
__device__ __forceinline__ void pace(u32 lim) {
  u32 a;
  asm volatile("1:\n\ts_getreg_b32 %0, hwreg(HW_REG_IB_STS, 0, 4)\n\ts_memrealtime vcc\n\ts_max_u32 %0, %0, %2\n\ts_mulk_i32 %0, %3\n\ts_add_u32 %0, %0, %1\n\t"
               "s_waitcnt lgkmcnt(0)\n\ts_sub_u32 %0, vcc_lo, %0\n\ts_cmp_lt_i32 %0, 0\n\ts_cbranch_scc1 1b"
               : "=&s"(a) : "s"(lim), "n"(N0), "n"(CC) : "vcc", "scc", "memory");
}

__global__ void __launch_bounds__(512, 2)
fused_decode(const u16* x, const u16* kc, const u16* vc, const u16* wq, const u16* wo, const u16* wg, const u16* wu, const u16* wd,
             u16* out, unsigned char* ws) {
  __shared__ __attribute__((aligned(16))) unsigned char lds[L_END];
  u64 pc; u32 clen;
  asm volatile("s_getpc_b64 %0\n.Lcode_lo:\n\ts_mov_b32 %1, .Lcode_hi-.Lcode_lo" : "=s"(pc), "=s"(clen));
  const u32 cpf = raw_load_b32(v4u{(u32)pc, (u32)(pc >> 32) & 0xffffu, clen, 0x00020000u}, threadIdx.x * 2048 + blockIdx.x * 8, 0, 0);
  PIN();
  const int tid = threadIdx.x, l = tid & 63, w = __builtin_amdgcn_readfirstlane(tid >> 6);
  const int bid = blockIdx.x, xg = bid & 7, idx = bid >> 3;
  const int r = l >> 3, c = l & 7, g = l >> 4, col = l & 15;
  const bool pf = idx >= 26;
  const int ks = idx & 1, pu = idx >> 1, pcol = pu * 8 + xg - (pf ? 104 : 0);
  const u32 pstr = pf ? F_ * 2 : HD * 2;
  const u32 op = (u32)(r * 10 + 320 * ks + 80 * w) * pstr + (u32)(pcol * 128 + c * 16) + (pu == 12 ? OOB : 0u);
  const u32 oxl = (u32)(tid & 0x13f) * 40u;
  u32 vzero;
  asm("v_mov_b32 %0, 0" : "=v"(vzero));
  asm volatile("" :: "s"(x), "s"(kc), "s"(vc), "s"(wq), "s"(wo), "s"(wg), "s"(wu), "s"(wd), "s"(out), "s"(ws), "v"(op), "v"(oxl));
  const rsrc_t rX = mk(x, 2 * D_ * 2);
  const rsrc_t rW = mk(ws, WS_END);
  const v4u xa = __builtin_amdgcn_raw_buffer_load_b128(rX, oxl, 0, 0);
  const v4u xb = __builtin_amdgcn_raw_buffer_load_b128(rX, oxl + 16, 0, 0);
  const v2u xc = __builtin_amdgcn_raw_buffer_load_b64(rX, oxl + 32, 0, 0);
  const u32 e = __builtin_amdgcn_raw_buffer_load_b32(rW, vzero, WS_E, AUX_SC1);
  PIN();
  int h, b, sp, G, lc;
  bool tile;
  auto setup = [&]() {
    int i2 = idx;
    asm volatile("" : "+s"(i2));
    h = ((i2 >> 2) & 3) * 8 + xg; b = i2 >> 4; sp = i2 & 3;
    tile = i2 < 30; G = xg * 3 + i2 / 10; lc = i2 % 10;
  };
  auto tag_of = [&](u32 ep) { return ep + ((((u32)dispatch_id() * 0x9E3779B1u) & 0x7e7e7e7eu) | 0x01010101u); };
  auto wait_flag = [&](int off, auto SL) {
    _Pragma("unroll 1") for (int spin = 0; spin < SPIN_CAP * 64 / (SL + 2); ++spin) {
      asm volatile("" ::: "memory");
      if (__builtin_amdgcn_readfirstlane(*(const u32*)(lds + off)) != SENT) break;
      __builtin_amdgcn_s_sleep(SL.value);
    }
    asm volatile("" ::: "memory");
  };

  if (w >= 4) {
    const int sw = w - 4;
    wg_barrier();
    setup();
    const int bh = b * H_ + h;
    const u32 okv = (u32)((sp * 32 + sw * 8 + r) * 384 + c * 16);
    auto kvstep = [&](int i) { return (u32)(((i + bh * ROT) & 31) * 49152); };
    const rsrc_t rK = mk((const unsigned char*)kc + (u32)(bh * (SK * DH * 2)), SK * DH * 2);
    const rsrc_t rV = mk((const unsigned char*)vc + (u32)(bh * (SK * DH * 2)), SK * DH * 2);
    v4u KV[64][3];
    u32 zt[3];
    {
      const u32 o3 = (u32)(l * 524288 - (l == 3 ? 4 : 0)) | (l < 4 ? 0u : OOB);
      zt[0] = __builtin_amdgcn_raw_buffer_load_b32(rK, o3 | (sw == 0 ? 0u : OOB), 0, AUX_NT);
      zt[1] = __builtin_amdgcn_raw_buffer_load_b32(rV, o3 | (sw == 1 ? 0u : OOB), 0, AUX_NT);
      zt[2] = __builtin_amdgcn_raw_buffer_load_b32(mk(wo, HD * D_ * 2), (u32)((256 * G + 255 * l) * (D_ * 2)) | ((l < 2 && sw == 2) ? 0u : OOB), 0, AUX_NT);
      PIN();
    }
    const int i0 = ((l + 1 - b) & ~1) + b - 1, i1 = i0 < 63 ? i0 + 1 : 63;
    const int tp = TOFF + (((i0 + i1) * 8 + ((bh >> 1) & 15) - 8) * TAU >> 8);
    const int tl = tp - N0 * CC, tm = tp + (C0 + 3) * (TAU / 48), otb = (i0 > 0 ? i0 : 0) * 4;
    u32 tb = *(const u32*)(lds + L_TB + l * 4);
    const u32 orep = l == 0 ? (u32)((b * 8 + xg) * PSTR) : OOB;
    const int rep = sw == 0 && idx == 5 ? 0 : sw == 0 && idx == 22 ? 1 : -1;
    auto iss = [&](auto I) {
      pace(__builtin_amdgcn_readlane(tb + tl, I));
      constexpr int J = (I + 1) & ~1;
      if constexpr (J >= CK0 && (J - CK0) % CKS == 0 && (J - CK0) / CKS < NCK) {
        if (rep == (I & 1)) {
          const u32 now = clk(), v = now + vm_out() * (TAU / 48) - __builtin_amdgcn_readlane(tm, I);
          if ((int)(now - __builtin_amdgcn_readlane(tb, I)) < TOFF + (J + CKD - RLEAD) * TAU / 16 - LATE)
            __builtin_amdgcn_raw_buffer_store_b64(v2u{v, tag_of(e)}, rW, orep, WS_P + (J - CK0) / CKS * 16 * PSTR, AUX_SC1);
        }
      }
      sfor<0, 3>([&](auto P) { KV[I][P] = __builtin_amdgcn_raw_buffer_load_b128(I < 32 ? rK : rV, okv + P * 128, kvstep(I), AUX_NT); });
      if constexpr (I >= CK0 + CKD - 2) tb = *(const u32*)(lds + L_TB + otb);
      PIN();
    };
    sfor<0, NPRE>(iss);
    asm volatile("" :: "v"(zt[0]), "v"(zt[1]), "v"(zt[2]), "v"(cpf), "v"(e));
    wait_flag(L_FQ, IC(1));
    v4u q[3];
    sfor<0, 3>([&](auto P) { q[P] = *(const v4u*)(lds + L_Q + P * 128 + c * 16); });
    PIN();
    float sc[32], mw;
    sfor<0, 32>([&](auto I) {
      float d0 = 0.f, d1 = 0.f, d2 = 0.f;
      sfor<0, 4>([&](auto E) { d0 = dot2(KV[I][0][E.value], q[0][E.value], d0); d1 = dot2(KV[I][1][E.value], q[1][E.value], d1); d2 = dot2(KV[I][2][E.value], q[2][E.value], d2); });
      sc[I] = rbf(rbf(sum8(d0 + d1 + d2)) * 0.07216878364870322f);
      mw = I == 0 ? sc[I] : fmaxf(mw, sc[I]);
      asm volatile("" : "+v"(sc[I]), "+v"(mw));
      PIN();
      sfor<issued_before(I), issued_before(I + 1)>(iss);
    });
    mw = fmaxf(mw, dpp<ROR8>(mw)); mw = max32(max16(mw));
    v4u msk;
    sfor<0, 4>([&](auto I) { msk[I.value] = (c >> 1) == I ? ((c & 1) ? 0xffff0000u : 0x0000ffffu) : 0u; });
    float lw = 0.f;
    u32 pp[32];
    v4f acc[3] = {{0, 0, 0, 0}, {0, 0, 0, 0}, {0, 0, 0, 0}};
    sfor<32, 64>([&](auto I) {
      if constexpr (I < 40) {
        sfor<(I - 32) * 4, (I - 31) * 4>([&](auto N) { const float p = ex2((sc[N] - mw) * LOG2E); lw += p; pp[N] = pk(p, p); asm volatile("" : "+v"(pp[N])); });
        asm volatile("" : "+v"(lw));
      }
      if constexpr (I == 39) {
        lw += dpp<ROR8>(lw); lw = sum32(sum16(lw));
        *(v2u*)(lds + L_ML + sw * 8) = v2u{nosent(mw), nosent(lw)};
      }
      const v4u a = msk & pp[I - 32];
      sfor<0, 3>([&](auto P) { acc[P] = mfma32(a, KV[I][P], acc[P]); });
      if constexpr (I < 63) asm volatile("" : "+v"(acc[0]), "+v"(acc[1]), "+v"(acc[2]));
      PIN();
      sfor<issued_before(I), issued_before(I + 1)>(iss);
      if constexpr (I == GSTEP) { *(u32*)(lds + L_FP) = 1u; PIN(); }
      if constexpr (I == 63 - WMIN) { *(u32*)(lds + L_FW) = 1u; PIN(); }
    });
    if ((g >> 1) == (col >> 3)) sfor<0, 3>([&](auto P) { *(v4f*)(lds + L_RO + ((2 * sw + (g >> 1)) * 192 + c * 8 + 4 * (g & 1)) * 4 + P * 256) = acc[P]; });
    wg_barrier();
    asm volatile(".Lcode_hi:");
    return;
  }

  v4u P0[10], P1[10], WO[8], F2[8];
  {
    const rsrc_t rP = mk(pf ? wg : wq, D_ * pstr);
    sfor<0, 10>([&](auto I) { P0[I] = __builtin_amdgcn_raw_buffer_load_b128(rP, op, I * pstr, AUX_NT); __builtin_amdgcn_sched_barrier(0); });
    PIN();
  }
  setup();
  if (tid < 3) *(v4u*)(lds + L_ML + l * 16) = v4u{SENT, SENT, SENT, SENT};
  if (w == 1) { asm volatile("" :: "v"(e)); PIN(); *(u32*)(lds + L_TB + l * 4) = clk(); }
  wg_barrier();
  auto issWO = [&](auto LO, auto HI) {
    const u32 o = (u32)((256 * G + 64 * w + 4 * r) * (D_ * 2) + lc * 128 + c * 16) | (tile ? 0u : OOB);
    sfor<LO, HI>([&](auto N) { WO[N] = __builtin_amdgcn_raw_buffer_load_b128(mk(wo, HD * D_ * 2), o, (N / 4 * 32 + N % 4) * (D_ * 2), AUX_NT); });
    PIN();
  };
  issWO(IC(0), IC(WE));
  if (pf) sfor<0, 10>([&](auto I) { P1[I] = __builtin_amdgcn_raw_buffer_load_b128(mk(wu, D_ * F_ * 2), op, I * (F_ * 2), AUX_NT); });
  PIN();
  const u32 T = tag_of(e);
  {
    const u32 xd[10] = {xa[0], xa[1], xa[2], xa[3], xb[0], xb[1], xb[2], xb[3], xc[0], xc[1]};
    float s = 0.f;
    sfor<0, 10>([&](auto I) { s += lo16(xd[I]) + hi16(xd[I]); });
    const float mean = half_sum(s) * (1.f / D_);
    float v = 0.f;
    sfor<0, 10>([&](auto I) { const float d0 = lo16(xd[I]) - mean, d1 = hi16(xd[I]) - mean; v += d0 * d0 + d1 * d1; });
    const float var = half_sum(v) * (1.f / D_);
    const float mb = rbf(mean), inv = __builtin_amdgcn_rcpf(rbf(__builtin_sqrtf(rbf(rbf(var) + 1e-5f))));
    sfor<0, 5>([&](auto I) {
      const u32 d0 = pk(lo16(xd[2 * I]) - mb, hi16(xd[2 * I]) - mb), d1 = pk(lo16(xd[2 * I + 1]) - mb, hi16(xd[2 * I + 1]) - mb);
      *(v2u*)(lds + L_XN + w * 2560 + l * 40 + I * 8) = v2u{pk(lo16(d0) * inv, hi16(d0) * inv), pk(lo16(d1) * inv, hi16(d1) * inv)};
    });
    PIN();
  }
  sfor<0, 8>([&](auto I) { F2[I] = __builtin_amdgcn_raw_buffer_load_b128(mk(wd, F_ * D_ * 2), (u32)((64 * G + 8 * r) * (D_ * 2) + lc * 128 + c * 16) | ((w == 2 && tile) ? 0u : OOB), I * (D_ * 2), AUX_NT); });
  PIN();
  const u32 t0 = __builtin_amdgcn_readfirstlane(*(const u32*)(lds + L_TB));
  auto wait_clock = [&](int ticks) { while ((int)(clk() - t0) < ticks) __builtin_amdgcn_s_sleep(2); };
  const int ab = col >> 3, ar = 2 * g + ((col >> 2) & 1);
  const u32 hm = (l & 1) ? 0xffff0000u : 0x0000ffffu;
  u32 m0 = (l & 2) ? 0u : hm, m1 = (l & 2) ? hm : 0u;
  auto gemv = [&](u32 d, v4u W, v4f& lo, v4f& hi) { const v2u A = {d & m0, d & m1}; lo = mfma16(A, v2u{W[0], W[1]}, lo); hi = mfma16(A, v2u{W[2], W[3]}, hi); };
  const bool dv = (g & 1) == (col >> 3);
  const u32 ld0 = (u32)((g & 1) * 512 + (g >> 1) * 256 + c * 32);
  auto put = [&](int slot, v4f lo, v4f hi) { if (dv) { *(v4f*)(lds + L_RS + slot * 1024 + ld0) = lo; *(v4f*)(lds + L_RS + slot * 1024 + ld0 + 16) = hi; } };
  auto sum4 = [&](int slot) {
    v4f t[4];
    sfor<0, 4>([&](auto I) { t[I] = *(const v4f*)(lds + L_RS + (slot + I) * 1024 + l * 16); });
    v4f s = (t[0] + t[1]) + (t[2] + t[3]);
    sfor<0, 4>([&](auto E) { s[E.value] = sum32(s[E.value]); });
    return s;
  };
  {
    u32 xv[5];
    sfor<0, 5>([&](auto I) { xv[I] = *(const u32*)(lds + L_XN + w * 2560 + ab * 1280 + (320 * ks + 80 * w + 10 * ar) * 2 + I * 4); });
    v4f lo = {0, 0, 0, 0}, hi = {0, 0, 0, 0};
    sfor<0, 10>([&](auto I) { gemv(dup<I>(xv[I / 2]), P0[I], lo, hi); });
    put(w, lo, hi);
    if (pf) {
      lo = v4f{0, 0, 0, 0}; hi = v4f{0, 0, 0, 0};
      sfor<0, 10>([&](auto I) { gemv(dup<I>(xv[I / 2]), P1[I], lo, hi); });
      put(4 + w, lo, hi);
    }
    asm volatile("" ::: "memory");
    u32 n = 0;
    if (l == 0) n = __hip_atomic_fetch_add((u32*)(lds + L_CNT), 1u, __ATOMIC_RELAXED, __HIP_MEMORY_SCOPE_WORKGROUP);
    if (__builtin_amdgcn_readfirstlane(n) == SENT + 3u) {
      asm volatile("" ::: "memory");
      const u32 o = pf ? (u32)(WS_F + (pcol * 2 + ks) * 1024 + l * 16)
                       : (u32)(WS_Q + ks * 49152 + (l >> 4) * 24576 + pcol * 256 + (l & 15) * 16);
      const u32 om = o | ((l < 32 && pu != 12) ? 0u : OOB);
      __builtin_amdgcn_raw_buffer_store_b128(enc4n(sum4(0), T, l & 1), rW, om, 0, AUX_SC1);
      if (pf) __builtin_amdgcn_raw_buffer_store_b128(enc4n(sum4(4), T, l & 1), rW, om, 512, AUX_SC1);
    }
    PIN();
  }
  const int me = h * 8 + b * 4 + sp;
  const bool fin = w == 0 && bid < 40;
  const int fq = (bid & 3) * 8 + c;
  const u32 ofin = (u32)(((bid >> 2) * 24 + r) * 512 + fq * 16);
  const int oidx = (fq >> 4) * D_ + (bid >> 2) * 64 + (fq & 15) * 4;
  v4f gate = {0, 0, 0, 0}, fw = {0, 0, 0, 0};
  v2u x4 = {0, 0};

  if (w == 3) {
    wait_clock(QT);
    const v4f s = gather2<49152, 48>(rW, (u32)(WS_Q + b * 24576 + h * 768 + l * 16), l, T);
    if (l < 48) *(v2u*)(lds + L_Q + l * 8) = v2u{pk(s[0], s[1]), pk(s[2], s[3])};
    asm volatile("" ::: "memory");
    if (l == 0) *(u32*)(lds + L_FQ) = 1u;
  } else if (w == 2) {
    if (tile) {
      wait_clock(FT);
      const v4f s = gather2<1024, 64>(rW, (u32)(WS_F + G * 2048 + l * 16), l, T);
      float hv[4];
      sfor<0, 4>([&](auto E) {
        const auto t = __builtin_amdgcn_permlane32_swap(asu(s[E.value]), asu(s[E.value]), false, false);
        const float gg = rbf(asf(t[0])), uu = rbf(asf(t[1]));
        const float z = 0.7978845608028654f * (gg + 0.044715f * gg * gg * gg);
        hv[E] = rbf(gg / (1.f + ex2(-2.f * LOG2E * z))) * uu;
      });
      if (l < 32) *(v2u*)(lds + L_H + l * 8) = v2u{pk(hv[0], hv[1]), pk(hv[2], hv[3])};
      const v4u hh = *(const v4u*)(lds + L_H + ab * 128 + ar * 16);
      v4f lo = {0, 0, 0, 0}, hi = {0, 0, 0, 0};
      sfor<0, 8>([&](auto I) { gemv(dup<I>(hh[I / 2]), F2[I], lo, hi); });
      put(8, lo, hi);
      v4f y = *(const v4f*)(lds + L_RS + 8 * 1024 + l * 16);
      sfor<0, 4>([&](auto E) { y[E.value] = sum32(y[E.value]); });
      __builtin_amdgcn_raw_buffer_store_b128(enc4(y, T), rW, l < 32 ? (u32)(WS_Y + (lc * 24 + G) * 512 + l * 16) : OOB, 0, AUX_SC1);
    }
  } else if (w == 1) {
    int base = 0, m1_ = 0, m2_ = 0;
    bool tilted = false;
    _Pragma("unroll 1") for (int k = 0; k < NCK; ++k) {
      const int S = CK0 + k * CKS, E = S + CKD, end = S < 32 ? 32 : 64, bS = __builtin_amdgcn_readlane(base, S);
      wait_clock(__builtin_amdgcn_readlane(base, E - RLEAD) + TOFF + (E - RLEAD) * TAU / 16);
      asm volatile("" ::: "memory");
      const v2u z = __builtin_amdgcn_raw_buffer_load_b64(rW, (u32)(WS_P + (k * 16 + col) * PSTR), 0, AUX_SC1);
      int v = (int)(z[0] - t0);
      if (z[1] != T || (u32)(v + 1000) > 2000u) v = bS + MISS;
      v += __builtin_amdgcn_mov_dpp(v, XOR1, 0xf, 0xf, true); v += __builtin_amdgcn_mov_dpp(v, XOR2, 0xf, 0xf, true);
      v += __builtin_amdgcn_mov_dpp(v, HMIR, 0xf, 0xf, true); v += __builtin_amdgcn_mov_dpp(v, ROR8, 0xf, 0xf, true);
      const int m = __builtin_amdgcn_readfirstlane(v) >> 4;
      if (S == 32) tilted = false;
      if (k == 0) base = l >= E ? m - (l > 32 ? (l - 32) * XV / 16 : 0) : 0;
      else {
        const int bl = m - __builtin_amdgcn_readlane(base, E + 3 < 63 ? E + 3 : 63);
        int d = 0;
        if (k == 1) d = bl - KEEP1;
        else if (!tilted && (S < 32 || S >= 40) && m - m2_ >= (S == 12 ? GK12 : S == 16 ? GK16 : S < 32 ? GK : GV) && m - bS >= LV) {
          d = bl + GROW * (CKD < end - S ? CKD : end - S) - REST;
          const int n2 = (l < end ? l : end) - E;
          base += n2 > 0 ? n2 * (2 * SIG + (S < 32 ? 0 : XV)) / 16 : 0;
          tilted = true;
        } else if (bl > LMAX - (S > 32 ? (S - 32) * USL / 16 : 0)) d = bl - KEEP;
        const int rp = RAMP * (l - (E - 1));
        if (d > 0) base += rp < 0 ? 0 : rp < d ? rp : d;
      }
      m2_ = m1_; m1_ = m;
      *(u32*)(lds + L_TB + l * 4) = t0 + base;
    }
  } else {
    wait_flag(L_FQ, IC(16));
    x4 = __builtin_amdgcn_raw_buffer_load_b64(rX, fin ? (u32)(oidx * 2) : OOB, 0, 0);
    {
      const u32 z = __builtin_amdgcn_raw_buffer_load_b32(rW, l < 6 ? (u32)(me * REC + R_O + l * 128) : (l < 10 && tile) ? (u32)(WS_S + (lc * 24 + G) * 512 + (l - 6) * 128) : OOB, 0, AUX_SC1);
      asm volatile("" :: "v"(z));
    }
    __builtin_amdgcn_raw_buffer_store_b64(x4, mk(out, 2 * D_ * 2), (fin && l < 8) ? (u32)(oidx * 2) : OOB, 0, AUX_SC1);
    v2u mlw;
    _Pragma("unroll 1") for (int spin = 0; spin < SPIN_CAP * 4; ++spin) {
      asm volatile("" ::: "memory");
      mlw = *(const v2u*)(lds + L_ML + (l & 3) * 8);
      if (__all(mlw[0] != SENT && mlw[1] != SENT)) break;
      __builtin_amdgcn_s_sleep(16);
    }
    float Mg = asf(mlw[0]); Mg = fmaxf(Mg, dpp<XOR1>(Mg)); Mg = fmaxf(Mg, dpp<XOR2>(Mg));
    float Lg = asf(mlw[1]) * ex2((asf(mlw[0]) - Mg) * LOG2E); Lg += dpp<XOR1>(Lg); Lg += dpp<XOR2>(Lg);
    __builtin_amdgcn_raw_buffer_store_b128(enc4(v4f{Mg, Lg, 0.f, 0.f}, T), rW, l < 8 ? (u32)(me * REC + R_ML + l * 16) : OOB, 0, AUX_SC1);
    if (fin) {
      const v4f y4 = final_sum<16>(rW, ofin, WS_Y, T);
      sfor<0, 4>([&](auto E) { const float y = rbf(y4[E.value]); gate[E.value] = rbf(y / (1.f + ex2(-LOG2E * y))); });
    }
    wait_flag(L_FP, IC(16));
    v4u ml4;
    SPIN {
      asm volatile("" ::: "memory");
      ml4 = __builtin_amdgcn_raw_buffer_load_b128(rW, (u32)((h * 8 + b * 4 + (l & 3)) * REC + R_ML + ((l >> 2) & 7) * 16), 0, AUX_SC1);
      if (__all(tag4(ml4) == T)) break;
      __builtin_amdgcn_s_sleep(16);
    }
    float M = dec(ml4[0]); M = fmaxf(M, dpp<XOR1>(M)); M = fmaxf(M, dpp<XOR2>(M));
    float L = dec(ml4[1]) * ex2((dec(ml4[0]) - M) * LOG2E); L += dpp<XOR1>(L); L += dpp<XOR2>(L);
    const float f = ex2((asf(mlw[0]) - M) * LOG2E) / L;
    fw = v4f{dpp<0x00>(f), dpp<0x55>(f), dpp<0xAA>(f), dpp<0xFF>(f)};
  }

  asm volatile("" :: "v"(cpf));
  __builtin_amdgcn_s_waitcnt(0x0F70);
  const int R0 = 256 * G + 64 * w, hh = R0 / DH;
  u32 oo = (u32)((hh * 8 + ab * 4 + (l & 3)) * REC + R_O + (R0 - hh * DH + 4 * ar) * 4);
  u32 ost = l < 48 ? (u32)(me * REC + R_O + l * 16) : OOB, oss = l < 32 ? (u32)(WS_S + (lc * 24 + G) * 512 + l * 16) : OOB;
  u32 oout = l < 8 ? (u32)(oidx * 2) : OOB, oep = (bid == 0 && l == 0) ? (u32)WS_E : OOB;
  u32 lrs = dv ? (u32)(L_RS + w * 1024) + ld0 : (u32)(L_SP + l * 32);
  v4f xf = {lo16(x4[0]), hi16(x4[0]), lo16(x4[1]), hi16(x4[1])};
  asm volatile("" : "+v"(oo), "+v"(m0), "+v"(m1), "+v"(ost), "+v"(oss), "+v"(oout), "+v"(oep), "+v"(lrs), "+v"(xf), "+v"(fw), "+v"(gate));
  wait_flag(L_FW, IC(2));
  {
    const u32 tw = __builtin_amdgcn_readfirstlane(*(const u32*)(lds + L_TB + 63 * 4)) + (TOFF + 63 * TAU / 16 + WOT);
    while ((int)(clk() - tw) < 0) __builtin_amdgcn_s_sleep(1);
  }
  issWO(IC(WE), IC(8));
  wg_barrier();
  if (w == 0) {
    v4f t[8];
    sfor<0, 8>([&](auto I) { t[I] = *(const v4f*)(lds + L_RO + I * 768 + l * 16); });
    __builtin_amdgcn_sched_barrier(0);
    const v4f s = ((t[0] + t[1]) * fw[0] + (t[2] + t[3]) * fw[1]) + ((t[4] + t[5]) * fw[2] + (t[6] + t[7]) * fw[3]);
    __builtin_amdgcn_raw_buffer_store_b128(enc4(s, T), rW, ost, 0, AUX_SC1);
  } else {
    __builtin_amdgcn_s_sleep(S1 - S0);
  }
  if (tile) {
    __builtin_amdgcn_s_sleep(S0);
    v4u o4[2];
    SPIN {
      asm volatile("" ::: "memory");
      o4[0] = __builtin_amdgcn_raw_buffer_load_b128(rW, oo, 0, AUX_SC1);
      o4[1] = __builtin_amdgcn_raw_buffer_load_b128(rW, oo + 128, 0, AUX_SC1);
      if (__all(((tag4(o4[0]) ^ T) | (tag4(o4[1]) ^ T)) == 0u)) break;
    }
    float v[8];
    sfor<0, 8>([&](auto E) { v[E] = dec(o4[E / 4][E % 4]); });
    __builtin_amdgcn_sched_barrier(0);
    sfor<0, 8>([&](auto E) { v[E] += dpp<XOR1>(v[E]); });
    __builtin_amdgcn_sched_barrier(0);
    sfor<0, 8>([&](auto E) { v[E] += dpp<XOR2>(v[E]); });
    __builtin_amdgcn_sched_barrier(0);
    v2u A[8];
    auto build = [&](auto E) { const u32 d = pk(v[E], v[E]); A[E] = v2u{d & m0, d & m1}; };
    build(IC(0));
    v4f lo = {0, 0, 0, 0}, hi = {0, 0, 0, 0};
    sfor<0, 8>([&](auto E) {
      if constexpr (E < 7) build(IC(E + 1));
      __builtin_amdgcn_sched_barrier(0);
      lo = mfma16(A[E], v2u{WO[E][0], WO[E][1]}, lo); hi = mfma16(A[E], v2u{WO[E][2], WO[E][3]}, hi);
      __builtin_amdgcn_sched_barrier(0);
    });
    *(v4f*)(lds + lrs) = lo; *(v4f*)(lds + lrs + 16) = hi;
    wg_barrier();
    if (w == 0) {
      __builtin_amdgcn_raw_buffer_store_b128(enc4(sum4(0), T), rW, oss, 0, AUX_SC1);
      if (fin) {
        __builtin_amdgcn_s_sleep(S2);
        const v4f s4 = final_sum<0>(rW, ofin, WS_S, T);
        float o[4];
        sfor<0, 4>([&](auto E) { o[E] = xf[E.value] + rbf(rbf(s4[E.value]) * gate[E.value]); });
        __builtin_amdgcn_raw_buffer_store_b64(v2u{pk(o[0], o[1]), pk(o[2], o[3])}, mk(out, 2 * D_ * 2), oout, 0, AUX_SC1);
        __builtin_amdgcn_raw_buffer_store_b32(T, rW, oep, 0, AUX_SC1);
      }
    }
  }
}

extern "C" void kernel_launch(void* const* d_in, const int* in_sizes, int n_in, void* d_out, int out_size, void* d_ws, size_t ws_size,
                              hipStream_t stream) {
  fused_decode<<<256, 512, 0, stream>>>((const u16*)d_in[0], (const u16*)d_in[1], (const u16*)d_in[2], (const u16*)d_in[3], (const u16*)d_in[4],
                                        (const u16*)d_in[6], (const u16*)d_in[7], (const u16*)d_in[8], (u16*)d_out, (unsigned char*)d_ws);
}
```

```cpp
#include <hip/hip_runtime.h>
#include <stdint.h>
#include <type_traits>

typedef unsigned u32;
typedef unsigned long long u64;
typedef unsigned short u16;
typedef u32 v4u __attribute__((ext_vector_type(4)));
typedef u32 v2u __attribute__((ext_vector_type(2)));
typedef float v4f __attribute__((ext_vector_type(4)));
typedef short s4 __attribute__((ext_vector_type(4)));
typedef __bf16 bf8 __attribute__((ext_vector_type(8)));
typedef __bf16 bf2 __attribute__((ext_vector_type(2)));
typedef __amdgpu_buffer_rsrc_t rsrc_t;

constexpr int D_ = 640, H_ = 32, SK = 4096, DH = 192, F_ = 1536, HD = H_ * DH;
constexpr int AUX_NT = 2, AUX_SC1 = 16;
constexpr u32 OOB = 0x80000000u;
constexpr u32 SENT = 0xFFFFFFFFu;
constexpr int SPIN_CAP = 1 << 18;

constexpr int WS_E = 0;
constexpr int REC = 896, R_O = 256, R_ML = R_O + 768;
constexpr int WS_Q = R_O + 256 * REC;
constexpr int WS_F = WS_Q + 64 * 1536;
constexpr int WS_Y = WS_F + 48 * 1024;
constexpr int WS_S = WS_Y + 240 * 512;
constexpr int WS_P = WS_S + 240 * 512, PSTR = 32;
constexpr int WS_END = WS_P + 16 * 16 * PSTR;

constexpr int NPRE = 14, WMIN = 6;
constexpr int QT = 410, FT = 1300;
constexpr int GSTEP = 49;
constexpr int TAU = 665, XV = 14;
constexpr int TOFF = 190;
constexpr int N0 = 6, CC = 40;
constexpr int ROT = 7;
constexpr int WE = 4, WOT = 55;
constexpr int S0 = 8, S1 = 12, S2 = 12;
constexpr int CK0 = 4, CKS = 4, NCK = 14;
constexpr int CKD = 7, RLEAD = 3;
constexpr int C0 = 2, LATE = 90, MISS = 130;
constexpr int KEEP1 = 28, LMAX = 46, USL = 8, KEEP = 18;
constexpr int GK12 = 68, GK16 = 40, GK = 30, GV = 14, LV = 26, GROW = 3, REST = 5, SIG = 23, RAMP = 16;

#define IC(n) std::integral_constant<int, (n)>{}

constexpr int L_XN = 0;
constexpr int L_Q = L_XN + 4 * 2560;
constexpr int L_H = L_Q + 384;
constexpr int L_ML = L_H + 256;
constexpr int L_FQ = L_ML + 32, L_FP = L_ML + 36, L_CNT = L_ML + 40, L_FW = L_ML + 44;
constexpr int L_TB = L_ML + 64;
constexpr int L_RO = L_TB + 256;
constexpr int L_RS = L_RO + 8 * 768;
constexpr int L_SP = L_RS + 9 * 1024;
constexpr int L_END = L_SP + 2048;

template <int I, int N, class F> __device__ __forceinline__ void sfor(F&& f) {
  if constexpr (I < N) { f(std::integral_constant<int, I>{}); sfor<I + 1, N>(f); }
}
#define PIN() do { asm volatile("" ::: "memory"); __builtin_amdgcn_sched_barrier(0); } while (0)
#define SPIN _Pragma("unroll 1") for (int spin = 0; spin < SPIN_CAP; ++spin)
__device__ __forceinline__ void wg_barrier() {
  __builtin_amdgcn_sched_barrier(0);
  asm volatile("s_waitcnt lgkmcnt(0)\n\ts_barrier" ::: "memory");
  __builtin_amdgcn_sched_barrier(0);
}
__device__ __forceinline__ rsrc_t mk(const void* p, u32 bytes) { return __builtin_amdgcn_make_buffer_rsrc((void*)p, 0, bytes, 0x00020000); }
__device__ __forceinline__ float asf(u32 x) { return __builtin_bit_cast(float, x); }
__device__ __forceinline__ u32 asu(float x) { return __builtin_bit_cast(u32, x); }
__device__ __forceinline__ u32 pk(float lo, float hi) { bf2 v = {(__bf16)lo, (__bf16)hi}; return __builtin_bit_cast(u32, v); }
__device__ __forceinline__ float rbf(float x) { return (float)(__bf16)x; }
__device__ __forceinline__ float lo16(u32 x) { return asf(x << 16); }
__device__ __forceinline__ float hi16(u32 x) { return asf(x & 0xffff0000u); }
template <int C> __device__ __forceinline__ float dpp(float x) { return asf(__builtin_amdgcn_mov_dpp(asu(x), C, 0xf, 0xf, true)); }
constexpr int XOR1 = 0xB1, XOR2 = 0x4E, HMIR = 0x141, ROR8 = 0x128;
__device__ __forceinline__ float sum16(float x) { auto s = __builtin_amdgcn_permlane16_swap(asu(x), asu(x), false, false); return asf(s[0]) + asf(s[1]); }
__device__ __forceinline__ float sum32(float x) { auto s = __builtin_amdgcn_permlane32_swap(asu(x), asu(x), false, false); return asf(s[0]) + asf(s[1]); }
__device__ __forceinline__ float max16(float x) { auto s = __builtin_amdgcn_permlane16_swap(asu(x), asu(x), false, false); return fmaxf(asf(s[0]), asf(s[1])); }
__device__ __forceinline__ float max32(float x) { auto s = __builtin_amdgcn_permlane32_swap(asu(x), asu(x), false, false); return fmaxf(asf(s[0]), asf(s[1])); }
__device__ __forceinline__ float sum8(float x) { x += dpp<XOR1>(x); x += dpp<XOR2>(x); x += dpp<HMIR>(x); return x; }
__device__ __forceinline__ float half_sum(float x) { x = sum8(x); x += dpp<ROR8>(x); return sum16(x); }
__device__ __forceinline__ float dot2(u32 a, u32 b, float c) { return __builtin_amdgcn_fdot2_f32_bf16(__builtin_bit_cast(bf2, a), __builtin_bit_cast(bf2, b), c, false); }
__device__ __forceinline__ v4f mfma32(v4u a, v4u b, v4f c) { return __builtin_amdgcn_mfma_f32_16x16x32_bf16(__builtin_bit_cast(bf8, a), __builtin_bit_cast(bf8, b), c, 0, 0, 0); }
__device__ __forceinline__ v4f mfma16(v2u a, v2u b, v4f c) { return __builtin_amdgcn_mfma_f32_16x16x16bf16_1k(__builtin_bit_cast(s4, a), __builtin_bit_cast(s4, b), c, 0, 0, 0); }
template <int N> __device__ __forceinline__ u32 dup(u32 x) { return __builtin_amdgcn_perm(x, x, (N & 1) ? 0x03020302u : 0x01000100u); }
__device__ __forceinline__ u32 nosent(float x) { u32 u = asu(x); return u < 0xFFFFFFFEu ? u : 0xFFFFFFFEu; }
__device__ __forceinline__ float ex2(float x) { return __builtin_amdgcn_exp2f(x); }
constexpr float LOG2E = 1.4426950408889634f;

__device__ __forceinline__ v4u enc4(v4f x, u32 T) {
  return v4u{__builtin_amdgcn_perm(asu(x[0]), T, 0x07060500u), __builtin_amdgcn_perm(asu(x[1]), T, 0x07060501u),
             __builtin_amdgcn_perm(asu(x[2]), T, 0x07060502u), __builtin_amdgcn_perm(asu(x[3]), T, 0x07060503u)};
}
__device__ __forceinline__ u32 tag4(v4u x) { return __builtin_amdgcn_perm(x[1], x[0], 0x0c0c0400u) | __builtin_amdgcn_perm(x[3], x[2], 0x04000c0cu); }
__device__ __forceinline__ u32 tagdiff(v4u x, u32 T) { return __builtin_amdgcn_sad_u8(tag4(x), T, 0u); }
__device__ __forceinline__ float dec(u32 x) { return asf(x & 0xffffff00u); }
__device__ __forceinline__ v4u nibbles(u32 T, int odd) { const u32 t = T >> (16 * odd); return v4u{t & 15u, (t >> 4) & 15u, (t >> 8) & 15u, (t >> 12) & 15u}; }
__device__ __forceinline__ v4u enc4n(v4f x, u32 T, int odd) { return (__builtin_bit_cast(v4u, x) & 0xfffffff0u) | nibbles(T, odd); }
template <int STRIDE, int LANES> __device__ __forceinline__ v4f gather2(rsrc_t rW, u32 off, int l, u32 T) {
  v4u a, b;
  const v4u nb = nibbles(T, l & 1);
  SPIN {
    asm volatile("" ::: "memory");
    a = __builtin_amdgcn_raw_buffer_load_b128(rW, l < LANES ? off : OOB, 0, AUX_SC1);
    b = __builtin_amdgcn_raw_buffer_load_b128(rW, l < LANES ? off : OOB, STRIDE, AUX_SC1);
    const v4u m = (a ^ nb) | (b ^ nb);
    if (__all(l >= LANES || ((m[0] | m[1] | m[2] | m[3]) & 15u) == 0u)) break;
    __builtin_amdgcn_s_sleep(16);
  }
  return __builtin_bit_cast(v4f, a & 0xfffffff0u) + __builtin_bit_cast(v4f, b & 0xfffffff0u);
}
template <int SLEEP> __device__ __forceinline__ float final_sum(rsrc_t rW, u32 off, int base, u32 T) {
  v4u t[3];
  SPIN {
    asm volatile("" ::: "memory");
    sfor<0, 3>([&](auto I) { t[I] = __builtin_amdgcn_raw_buffer_load_b128(rW, off, base + I * 8 * 512, AUX_SC1); });
    if (__builtin_amdgcn_ballot_w64(tagdiff(t[0], T) + tagdiff(t[1], T) + tagdiff(t[2], T) != 0u) == 0ull) break;
    if (SLEEP) __builtin_amdgcn_s_sleep(SLEEP);
  }
  const v4f s = __builtin_bit_cast(v4f, t[0] & 0xffffff00u) + __builtin_bit_cast(v4f, t[1] & 0xffffff00u) + __builtin_bit_cast(v4f, t[2] & 0xffffff00u);
  const auto a = __builtin_amdgcn_permlane32_swap(asu(s[0]), asu(s[2]), false, false), b = __builtin_amdgcn_permlane32_swap(asu(s[1]), asu(s[3]), false, false);
  const auto d = __builtin_amdgcn_permlane16_swap(asu(asf(a[0]) + asf(a[1])), asu(asf(b[0]) + asf(b[1])), false, false);
  const float v = asf(d[0]) + asf(d[1]);
  return v + dpp<ROR8>(v);
}
constexpr int win_of(int u) { return NPRE - u / 3 > WMIN ? NPRE - u / 3 : WMIN; }
constexpr int issued_before(int u) { return u + win_of(u) < 64 ? u + win_of(u) : 64; }
extern "C" __device__ u32 raw_load_b32(v4u, u32, u32, u32) __asm("llvm.amdgcn.raw.buffer.load.i32");
extern "C" __device__ u64 dispatch_id() __asm("llvm.amdgcn.dispatch.id");
__device__ __forceinline__ u32 clk() { return (u32)__builtin_amdgcn_s_memrealtime(); }
__device__ __forceinline__ u32 vm_out() { return __builtin_amdgcn_s_getreg(7 | (3 << 11)); }
__device__ __forceinline__ void pace(u32 lim, u32& now) {
  u32 a;
  asm volatile("1:\n\ts_getreg_b32 %0, hwreg(HW_REG_IB_STS, 0, 4)\n\ts_max_u32 %0, %0, %3\n\ts_mulk_i32 %0, %4\n\ts_add_u32 %0, %0, %2\n\t"
               "s_sub_u32 %0, %1, %0\n\ts_cmp_ge_i32 %0, 0\n\ts_cbranch_scc1 2f\n\t"
               "s_memrealtime vcc\n\ts_waitcnt lgkmcnt(0)\n\ts_mov_b32 %1, vcc_lo\n\ts_branch 1b\n2:"
               : "=&s"(a), "+s"(now) : "s"(lim), "n"(N0), "n"(CC) : "vcc", "scc", "memory");
}

__global__ void __launch_bounds__(512, 2)
fused_decode(const u16* x, const u16* kc, const u16* vc, const u16* wq, const u16* wo, const u16* wg, const u16* wu, const u16* wd,
             u16* out, unsigned char* ws) {
  __shared__ __attribute__((aligned(16))) unsigned char lds[L_END];
  u64 pc; u32 clen;
  asm volatile("s_getpc_b64 %0\n.Lcode_lo:\n\ts_mov_b32 %1, .Lcode_hi-.Lcode_lo" : "=s"(pc), "=s"(clen));
  const u32 cpf = raw_load_b32(v4u{(u32)pc, (u32)(pc >> 32) & 0xffffu, clen, 0x00020000u}, threadIdx.x * 2048 + blockIdx.x * 8, 0, 0);
  PIN();
  const int tid = threadIdx.x, l = tid & 63, w = __builtin_amdgcn_readfirstlane(tid >> 6);
  const int bid = blockIdx.x, xg = bid & 7, idx = bid >> 3;
  const int r = l >> 3, c = l & 7, g = l >> 4, col = l & 15;
  const bool pf = idx >= 26;
  const int ks = idx & 1, pu = idx >> 1, pcol = pu * 8 + xg - (pf ? 104 : 0);
  const u32 pstr = pf ? F_ * 2 : HD * 2;
  const u32 op = (u32)(r * 10 + 320 * ks + 80 * w) * pstr + (u32)(pcol * 128 + c * 16) + (pu == 12 ? OOB : 0u);
  const u32 oxl = (u32)(tid & 0x13f) * 40u;
  u32 vzero;
  asm("v_mov_b32 %0, 0" : "=v"(vzero));
  asm volatile("" :: "s"(x), "s"(kc), "s"(vc), "s"(wq), "s"(wo), "s"(wg), "s"(wu), "s"(wd), "s"(out), "s"(ws), "v"(op), "v"(oxl));
  const rsrc_t rX = mk(x, 2 * D_ * 2);
  const rsrc_t rW = mk(ws, WS_END);
  const v4u xa = __builtin_amdgcn_raw_buffer_load_b128(rX, oxl, 0, 0);
  const v4u xb = __builtin_amdgcn_raw_buffer_load_b128(rX, oxl + 16, 0, 0);
  const v2u xc = __builtin_amdgcn_raw_buffer_load_b64(rX, oxl + 32, 0, 0);
  const u32 e = __builtin_amdgcn_raw_buffer_load_b32(rW, vzero, WS_E, AUX_SC1);
  PIN();
  int h, b, sp, G, lc;
  bool tile;
  auto setup = [&]() {
    int i2 = idx;
    asm volatile("" : "+s"(i2));
    h = ((i2 >> 2) & 3) * 8 + xg; b = i2 >> 4; sp = i2 & 3;
    tile = i2 < 30; G = xg * 3 + i2 / 10; lc = i2 % 10;
  };
  auto tag_of = [&](u32 ep) { return ep + ((((u32)dispatch_id() * 0x9E3779B1u) & 0x7e7e7e7eu) | 0x01010101u); };
  auto wait_flag = [&](int off, auto SL) {
    _Pragma("unroll 1") for (int spin = 0; spin < SPIN_CAP * 64 / (SL + 2); ++spin) {
      asm volatile("" ::: "memory");
      if (__builtin_amdgcn_readfirstlane(*(const u32*)(lds + off)) != SENT) break;
      __builtin_amdgcn_s_sleep(SL.value);
    }
    asm volatile("" ::: "memory");
  };

  if (w >= 4) {
    const int sw = w - 4;
    wg_barrier();
    setup();
    const int bh = b * H_ + h;
    const u32 okv = (u32)((sp * 32 + sw * 8 + r) * 384 + c * 16);
    auto kvstep = [&](int i) { return (u32)(((i + bh * ROT) & 31) * 49152); };
    const rsrc_t rK = mk((const unsigned char*)kc + (u32)(bh * (SK * DH * 2)), SK * DH * 2);
    const rsrc_t rV = mk((const unsigned char*)vc + (u32)(bh * (SK * DH * 2)), SK * DH * 2);
    v4u KV[64][3];
    u32 zt[3];
    {
      const u32 o3 = (u32)(l * 524288 - (l == 3 ? 4 : 0)) | (l < 4 ? 0u : OOB);
      zt[0] = __builtin_amdgcn_raw_buffer_load_b32(rK, o3 | (sw == 0 ? 0u : OOB), 0, AUX_NT);
      zt[1] = __builtin_amdgcn_raw_buffer_load_b32(rV, o3 | (sw == 1 ? 0u : OOB), 0, AUX_NT);
      zt[2] = __builtin_amdgcn_raw_buffer_load_b32(mk(wo, HD * D_ * 2), (u32)((256 * G + 255 * l) * (D_ * 2)) | ((l < 2 && sw == 2) ? 0u : OOB), 0, AUX_NT);
      PIN();
    }
    const int i0 = ((l + 1 - b) & ~1) + b - 1, i1 = i0 < 63 ? i0 + 1 : 63;
    const int tp = TOFF + (((i0 + i1) * 8 + ((bh >> 1) & 15) - 8) * TAU >> 8);
    const int tl = tp - N0 * CC, tm = tp + (C0 + 3) * (TAU / 48), otb = (i0 > 0 ? i0 : 0) * 4;
    u32 tb = *(const u32*)(lds + L_TB + l * 4);
    u32 tnow = clk();
    const u32 orep = l == 0 ? (u32)((b * 8 + xg) * PSTR) : OOB;
    const int rep = sw == 0 && idx == 5 ? 0 : sw == 0 && idx == 22 ? 1 : -1;
    auto iss = [&](auto I) {
      pace(__builtin_amdgcn_readlane(tb + tl, I), tnow);
      constexpr int J = (I + 1) & ~1;
      if constexpr (J >= CK0 && (J - CK0) % CKS == 0 && (J - CK0) / CKS < NCK) {
        if (rep == (I & 1)) {
          const u32 now = clk(), v = now + vm_out() * (TAU / 48) - __builtin_amdgcn_readlane(tm, I);
          if ((int)(now - __builtin_amdgcn_readlane(tb, I)) < TOFF + (J + CKD - RLEAD) * TAU / 16 - LATE)
            __builtin_amdgcn_raw_buffer_store_b64(v2u{v, tag_of(e)}, rW, orep, WS_P + (J - CK0) / CKS * 16 * PSTR, AUX_SC1);
        }
      }
      sfor<0, 3>([&](auto P) { KV[I][P] = __builtin_amdgcn_raw_buffer_load_b128(I < 32 ? rK : rV, okv + P * 128, kvstep(I), AUX_NT); });
      if constexpr (I >= CK0 + CKD - 2) tb = *(const u32*)(lds + L_TB + otb);
      PIN();
    };
    sfor<0, NPRE>(iss);
    asm volatile("" :: "v"(zt[0]), "v"(zt[1]), "v"(zt[2]), "v"(cpf), "v"(e));
    wait_flag(L_FQ, IC(1));
    v4u q[3];
    sfor<0, 3>([&](auto P) { q[P] = *(const v4u*)(lds + L_Q + P * 128 + c * 16); });
    PIN();
    float sc[32], mw;
    sfor<0, 32>([&](auto I) {
      float d0 = 0.f, d1 = 0.f, d2 = 0.f;
      sfor<0, 4>([&](auto E) { d0 = dot2(KV[I][0][E.value], q[0][E.value], d0); d1 = dot2(KV[I][1][E.value], q[1][E.value], d1); d2 = dot2(KV[I][2][E.value], q[2][E.value], d2); });
      sc[I] = rbf(rbf(sum8(d0 + d1 + d2)) * 0.07216878364870322f);
      mw = I == 0 ? sc[I] : fmaxf(mw, sc[I]);
      asm volatile("" : "+v"(sc[I]), "+v"(mw));
      PIN();
      sfor<issued_before(I), issued_before(I + 1)>(iss);
    });
    mw = fmaxf(mw, dpp<ROR8>(mw)); mw = max32(max16(mw));
    v4u msk;
    sfor<0, 4>([&](auto I) { msk[I.value] = (c >> 1) == I ? ((c & 1) ? 0xffff0000u : 0x0000ffffu) : 0u; });
    float lw = 0.f;
    u32 pp[32];
    v4f acc[3] = {{0, 0, 0, 0}, {0, 0, 0, 0}, {0, 0, 0, 0}};
    sfor<32, 64>([&](auto I) {
      if constexpr (I < 40) {
        sfor<(I - 32) * 4, (I - 31) * 4>([&](auto N) { const float p = ex2((sc[N] - mw) * LOG2E); lw += p; pp[N] = pk(p, p); asm volatile("" : "+v"(pp[N])); });
        asm volatile("" : "+v"(lw));
      }
      if constexpr (I == 39) {
        lw += dpp<ROR8>(lw); lw = sum32(sum16(lw));
        *(v2u*)(lds + L_ML + sw * 8) = v2u{nosent(mw), nosent(lw)};
      }
      const v4u a = msk & pp[I - 32];
      sfor<0, 3>([&](auto P) { acc[P] = mfma32(a, KV[I][P], acc[P]); });
      if constexpr (I < 63) asm volatile("" : "+v"(acc[0]), "+v"(acc[1]), "+v"(acc[2]));
      PIN();
      sfor<issued_before(I), issued_before(I + 1)>(iss);
      if constexpr (I == GSTEP) { *(u32*)(lds + L_FP) = 1u; PIN(); }
      if constexpr (I == 63 - WMIN) { *(u32*)(lds + L_FW) = 1u; PIN(); }
    });
    if ((g >> 1) == (col >> 3)) sfor<0, 3>([&](auto P) { *(v4f*)(lds + L_RO + ((2 * sw + (g >> 1)) * 192 + c * 8 + 4 * (g & 1)) * 4 + P * 256) = acc[P]; });
    wg_barrier();
    asm volatile(".Lcode_hi:");
    return;
  }

  v4u P0[10], P1[10], WO[8], F2[8];
  {
    const rsrc_t rP = mk(pf ? wg : wq, D_ * pstr);
    sfor<0, 10>([&](auto I) { P0[I] = __builtin_amdgcn_raw_buffer_load_b128(rP, op, I * pstr, AUX_NT); __builtin_amdgcn_sched_barrier(0); });
    PIN();
  }
  setup();
  if (tid < 3) *(v4u*)(lds + L_ML + l * 16) = v4u{SENT, SENT, SENT, SENT};
  if (w == 1) { asm volatile("" :: "v"(e)); PIN(); *(u32*)(lds + L_TB + l * 4) = clk(); }
  wg_barrier();
  auto issWO = [&](auto LO, auto HI, bool on = true) {
    const u32 o = (u32)((256 * G + 64 * w + 4 * r) * (D_ * 2) + lc * 128 + c * 16) | (tile && on ? 0u : OOB);
    sfor<LO, HI>([&](auto N) { WO[N] = __builtin_amdgcn_raw_buffer_load_b128(mk(wo, HD * D_ * 2), o, (N / 4 * 32 + N % 4) * (D_ * 2), AUX_NT); });
    PIN();
  };
  issWO(IC(0), IC(WE), !pf);
  if (pf) sfor<0, 10>([&](auto I) { P1[I] = __builtin_amdgcn_raw_buffer_load_b128(mk(wu, D_ * F_ * 2), op, I * (F_ * 2), AUX_NT); });
  PIN();
  const u32 T = tag_of(e);
  {
    const u32 xd[10] = {xa[0], xa[1], xa[2], xa[3], xb[0], xb[1], xb[2], xb[3], xc[0], xc[1]};
    float s = 0.f;
    sfor<0, 10>([&](auto I) { s += lo16(xd[I]) + hi16(xd[I]); });
    const float mean = half_sum(s) * (1.f / D_);
    float v = 0.f;
    sfor<0, 10>([&](auto I) { const float d0 = lo16(xd[I]) - mean, d1 = hi16(xd[I]) - mean; v += d0 * d0 + d1 * d1; });
    const float var = half_sum(v) * (1.f / D_);
    const float mb = rbf(mean), inv = __builtin_amdgcn_rcpf(rbf(__builtin_sqrtf(rbf(rbf(var) + 1e-5f))));
    sfor<0, 5>([&](auto I) {
      const u32 d0 = pk(lo16(xd[2 * I]) - mb, hi16(xd[2 * I]) - mb), d1 = pk(lo16(xd[2 * I + 1]) - mb, hi16(xd[2 * I + 1]) - mb);
      *(v2u*)(lds + L_XN + w * 2560 + l * 40 + I * 8) = v2u{pk(lo16(d0) * inv, hi16(d0) * inv), pk(lo16(d1) * inv, hi16(d1) * inv)};
    });
    PIN();
  }
  sfor<0, 8>([&](auto I) { F2[I] = __builtin_amdgcn_raw_buffer_load_b128(mk(wd, F_ * D_ * 2), (u32)((64 * G + 8 * r) * (D_ * 2) + lc * 128 + c * 16) | ((w == 2 && tile) ? 0u : OOB), I * (D_ * 2), AUX_NT); });
  PIN();
  const u32 t0 = __builtin_amdgcn_readfirstlane(*(const u32*)(lds + L_TB));
  auto wait_clock = [&](int ticks) { while ((int)(clk() - t0) < ticks) __builtin_amdgcn_s_sleep(2); };
  const int ab = col >> 3, ar = 2 * g + ((col >> 2) & 1);
  const u32 hm = (l & 1) ? 0xffff0000u : 0x0000ffffu;
  u32 m0 = (l & 2) ? 0u : hm, m1 = (l & 2) ? hm : 0u;
  auto gemv = [&](u32 d, v4u W, v4f& lo, v4f& hi) { const v2u A = {d & m0, d & m1}; lo = mfma16(A, v2u{W[0], W[1]}, lo); hi = mfma16(A, v2u{W[2], W[3]}, hi); };
  const bool dv = (g & 1) == (col >> 3);
  const u32 ld0 = (u32)((g & 1) * 512 + (g >> 1) * 256 + c * 32);
  auto put = [&](int slot, v4f lo, v4f hi) { if (dv) { *(v4f*)(lds + L_RS + slot * 1024 + ld0) = lo; *(v4f*)(lds + L_RS + slot * 1024 + ld0 + 16) = hi; } };
  auto sum4 = [&](int slot) {
    v4f t[4];
    sfor<0, 4>([&](auto I) { t[I] = *(const v4f*)(lds + L_RS + (slot + I) * 1024 + l * 16); });
    v4f s = (t[0] + t[1]) + (t[2] + t[3]);
    sfor<0, 4>([&](auto E) { s[E.value] = sum32(s[E.value]); });
    return s;
  };
  {
    u32 xv[5];
    sfor<0, 5>([&](auto I) { xv[I] = *(const u32*)(lds + L_XN + w * 2560 + ab * 1280 + (320 * ks + 80 * w + 10 * ar) * 2 + I * 4); });
    v4f lo = {0, 0, 0, 0}, hi = {0, 0, 0, 0};
    sfor<0, 10>([&](auto I) { gemv(dup<I>(xv[I / 2]), P0[I], lo, hi); });
    put(w, lo, hi);
    if (pf) {
      lo = v4f{0, 0, 0, 0}; hi = v4f{0, 0, 0, 0};
      sfor<0, 10>([&](auto I) { gemv(dup<I>(xv[I / 2]), P1[I], lo, hi); });
      put(4 + w, lo, hi);
    }
    asm volatile("" ::: "memory");
    u32 n = 0;
    if (l == 0) n = __hip_atomic_fetch_add((u32*)(lds + L_CNT), 1u, __ATOMIC_RELAXED, __HIP_MEMORY_SCOPE_WORKGROUP);
    if (__builtin_amdgcn_readfirstlane(n) == SENT + 3u) {
      asm volatile("" ::: "memory");
      const u32 o = pf ? (u32)(WS_F + (pcol * 2 + ks) * 1024 + l * 16)
                       : (u32)(WS_Q + ks * 49152 + (l >> 4) * 24576 + pcol * 256 + (l & 15) * 16);
      const u32 om = o | ((l < 32 && pu != 12) ? 0u : OOB);
      __builtin_amdgcn_raw_buffer_store_b128(enc4n(sum4(0), T, l & 1), rW, om, 0, AUX_SC1);
      if (pf) __builtin_amdgcn_raw_buffer_store_b128(enc4n(sum4(4), T, l & 1), rW, om, 512, AUX_SC1);
    }
    PIN();
  }
  if (pf) issWO(IC(0), IC(WE));
  const int me = h * 8 + b * 4 + sp;
  const bool fin = w == 0 && bid < 40;
  const int fq = (bid & 3) * 8 + c;
  const u32 ofin = (u32)(((bid >> 2) * 24 + r) * 512 + fq * 16);
  const u32 oidx = (u32)(((fq >> 4) * D_ + (bid >> 2) * 64 + (fq & 15) * 4) * 2);
  u32 oout = (fin && l < 8) ? oidx : OOB;
  v4f fw = {0, 0, 0, 0};
  float gate = 0.f, xv = 0.f;

  if (w == 3) {
    wait_clock(QT);
    const v4f s = gather2<49152, 48>(rW, (u32)(WS_Q + b * 24576 + h * 768 + l * 16), l, T);
    if (l < 48) *(v2u*)(lds + L_Q + l * 8) = v2u{pk(s[0], s[1]), pk(s[2], s[3])};
    asm volatile("" ::: "memory");
    if (l == 0) *(u32*)(lds + L_FQ) = 1u;
  } else if (w == 2) {
    if (tile) {
      wait_clock(FT);
      const v4f s = gather2<1024, 64>(rW, (u32)(WS_F + G * 2048 + l * 16), l, T);
      float hv[4];
      sfor<0, 4>([&](auto E) {
        const auto t = __builtin_amdgcn_permlane32_swap(asu(s[E.value]), asu(s[E.value]), false, false);
        const float gg = rbf(asf(t[0])), uu = rbf(asf(t[1]));
        const float z = 0.7978845608028654f * (gg + 0.044715f * gg * gg * gg);
        hv[E] = rbf(gg / (1.f + ex2(-2.f * LOG2E * z))) * uu;
      });
      if (l < 32) *(v2u*)(lds + L_H + l * 8) = v2u{pk(hv[0], hv[1]), pk(hv[2], hv[3])};
      const v4u hh = *(const v4u*)(lds + L_H + ab * 128 + ar * 16);
      v4f lo = {0, 0, 0, 0}, hi = {0, 0, 0, 0};
      sfor<0, 8>([&](auto I) { gemv(dup<I>(hh[I / 2]), F2[I], lo, hi); });
      put(8, lo, hi);
      v4f y = *(const v4f*)(lds + L_RS + 8 * 1024 + l * 16);
      sfor<0, 4>([&](auto E) { y[E.value] = sum32(y[E.value]); });
      __builtin_amdgcn_raw_buffer_store_b128(enc4(y, T), rW, l < 32 ? (u32)(WS_Y + (lc * 24 + G) * 512 + l * 16) : OOB, 0, AUX_SC1);
    }
  } else if (w == 1) {
    int base = 0, m1_ = 0, m2_ = 0;
    bool tilted = false;
    _Pragma("unroll 1") for (int k = 0; k < NCK; ++k) {
      const int S = CK0 + k * CKS, E = S + CKD, end = S < 32 ? 32 : 64, bS = __builtin_amdgcn_readlane(base, S);
      wait_clock(__builtin_amdgcn_readlane(base, E - RLEAD) + TOFF + (E - RLEAD) * TAU / 16);
      asm volatile("" ::: "memory");
      const v2u z = __builtin_amdgcn_raw_buffer_load_b64(rW, (u32)(WS_P + (k * 16 + col) * PSTR), 0, AUX_SC1);
      int v = (int)(z[0] - t0);
      if (z[1] != T || (u32)(v + 1000) > 2000u) v = bS + MISS;
      v += __builtin_amdgcn_mov_dpp(v, XOR1, 0xf, 0xf, true); v += __builtin_amdgcn_mov_dpp(v, XOR2, 0xf, 0xf, true);
      v += __builtin_amdgcn_mov_dpp(v, HMIR, 0xf, 0xf, true); v += __builtin_amdgcn_mov_dpp(v, ROR8, 0xf, 0xf, true);
      const int m = __builtin_amdgcn_readfirstlane(v) >> 4;
      if (S == 32) tilted = false;
      if (k == 0) base = l >= E ? m - (l > 32 ? (l - 32) * XV / 16 : 0) : 0;
      else {
        const int bl = m - __builtin_amdgcn_readlane(base, E + 3 < 63 ? E + 3 : 63);
        int d = 0;
        if (k == 1) d = bl - KEEP1;
        else if (!tilted && (S < 32 || S >= 40) && m - m2_ >= (S == 12 ? GK12 : S == 16 ? GK16 : S < 32 ? GK : GV) && m - bS >= LV) {
          d = bl + GROW * (CKD < end - S ? CKD : end - S) - REST;
          const int n2 = (l < end ? l : end) - E;
          base += n2 > 0 ? n2 * (2 * SIG + (S < 32 ? 0 : XV)) / 16 : 0;
          tilted = true;
        } else if (bl > LMAX - (S > 32 ? (S - 32) * USL / 16 : 0)) d = bl - KEEP;
        const int rp = RAMP * (l - (E - 1));
        if (d > 0) base += rp < 0 ? 0 : rp < d ? rp : d;
      }
      m2_ = m1_; m1_ = m;
      *(u32*)(lds + L_TB + l * 4) = t0 + base;
    }
  } else {
    wait_flag(L_FQ, IC(16));
    xv = lo16(__builtin_amdgcn_raw_buffer_load_b16(rX, fin ? oidx + g * 2 : OOB, 0, 0));
    {
      const u32 z = __builtin_amdgcn_raw_buffer_load_b32(rW, l < 6 ? (u32)(me * REC + R_O + l * 128) : (l < 10 && tile) ? (u32)(WS_S + (lc * 24 + G) * 512 + (l - 6) * 128) : OOB, 0, AUX_SC1);
      asm volatile("" :: "v"(z));
    }
    __builtin_amdgcn_raw_buffer_store_b64(v2u{0u, 0u}, mk(out, 2 * D_ * 2), oout, 0, AUX_SC1);
    v2u mlw;
    _Pragma("unroll 1") for (int spin = 0; spin < SPIN_CAP * 4; ++spin) {
      asm volatile("" ::: "memory");
      mlw = *(const v2u*)(lds + L_ML + (l & 3) * 8);
      if (__all(mlw[0] != SENT && mlw[1] != SENT)) break;
      __builtin_amdgcn_s_sleep(16);
    }
    float Mg = asf(mlw[0]); Mg = fmaxf(Mg, dpp<XOR1>(Mg)); Mg = fmaxf(Mg, dpp<XOR2>(Mg));
    float Lg = asf(mlw[1]) * ex2((asf(mlw[0]) - Mg) * LOG2E); Lg += dpp<XOR1>(Lg); Lg += dpp<XOR2>(Lg);
    __builtin_amdgcn_raw_buffer_store_b128(enc4(v4f{Mg, Lg, 0.f, 0.f}, T), rW, l < 8 ? (u32)(me * REC + R_ML + l * 16) : OOB, 0, AUX_SC1);
    if (fin) {
      const float y = rbf(final_sum<16>(rW, ofin, WS_Y, T));
      gate = rbf(y / (1.f + ex2(-LOG2E * y)));
    }
    wait_flag(L_FP, IC(16));
    v4u ml4;
    SPIN {
      asm volatile("" ::: "memory");
      ml4 = __builtin_amdgcn_raw_buffer_load_b128(rW, (u32)((h * 8 + b * 4 + (l & 3)) * REC + R_ML + ((l >> 2) & 7) * 16), 0, AUX_SC1);
      if (__all(tag4(ml4) == T)) break;
      __builtin_amdgcn_s_sleep(16);
    }
    float M = dec(ml4[0]); M = fmaxf(M, dpp<XOR1>(M)); M = fmaxf(M, dpp<XOR2>(M));
    float L = dec(ml4[1]) * ex2((dec(ml4[0]) - M) * LOG2E); L += dpp<XOR1>(L); L += dpp<XOR2>(L);
    const float f = ex2((asf(mlw[0]) - M) * LOG2E) / L;
    fw = v4f{dpp<0x00>(f), dpp<0x55>(f), dpp<0xAA>(f), dpp<0xFF>(f)};
  }

  asm volatile("" :: "v"(cpf));
  __builtin_amdgcn_s_waitcnt(0x0F70);
  const int R0 = 256 * G + 64 * w, hh = R0 / DH;
  u32 oo = (u32)((hh * 8 + ab * 4 + (l & 3)) * REC + R_O + (R0 - hh * DH + 4 * ar) * 4);
  u32 ost = l < 48 ? (u32)(me * REC + R_O + l * 16) : OOB, oss = l < 32 ? (u32)(WS_S + (lc * 24 + G) * 512 + l * 16) : OOB;
  u32 oep = (bid == 0 && l == 0) ? (u32)WS_E : OOB;
  u32 lrs = dv ? (u32)(L_RS + w * 1024) + ld0 : (u32)(L_SP + l * 32);
  asm volatile("" : "+v"(oo), "+v"(m0), "+v"(m1), "+v"(ost), "+v"(oss), "+v"(oout), "+v"(oep), "+v"(lrs), "+v"(xv), "+v"(fw), "+v"(gate));
  wait_flag(L_FW, IC(2));
  {
    const u32 tw = __builtin_amdgcn_readfirstlane(*(const u32*)(lds + L_TB + 63 * 4)) + (TOFF + 63 * TAU / 16 + WOT);
    while ((int)(clk() - tw) < 0) __builtin_amdgcn_s_sleep(1);
  }
  issWO(IC(WE), IC(8));
  wg_barrier();
  if (w == 0) {
    v4f t[8];
    sfor<0, 8>([&](auto I) { t[I] = *(const v4f*)(lds + L_RO + I * 768 + l * 16); });
    __builtin_amdgcn_sched_barrier(0);
    const v4f s = ((t[0] + t[1]) * fw[0] + (t[2] + t[3]) * fw[1]) + ((t[4] + t[5]) * fw[2] + (t[6] + t[7]) * fw[3]);
    __builtin_amdgcn_raw_buffer_store_b128(enc4(s, T), rW, ost, 0, AUX_SC1);
  } else {
    __builtin_amdgcn_s_sleep(S1 - S0);
  }
  if (tile) {
    __builtin_amdgcn_s_sleep(S0);
    v4u o4[2];
    SPIN {
      asm volatile("" ::: "memory");
      o4[0] = __builtin_amdgcn_raw_buffer_load_b128(rW, oo, 0, AUX_SC1);
      o4[1] = __builtin_amdgcn_raw_buffer_load_b128(rW, oo + 128, 0, AUX_SC1);
      if (__builtin_amdgcn_ballot_w64(tagdiff(o4[0], T) + tagdiff(o4[1], T) != 0u) == 0ull) break;
    }
    float v[8];
    sfor<0, 8>([&](auto E) { v[E] = dec(o4[E / 4][E % 4]); });
    __builtin_amdgcn_sched_barrier(0);
    sfor<0, 8>([&](auto E) { v[E] += dpp<XOR1>(v[E]); });
    __builtin_amdgcn_sched_barrier(0);
    sfor<0, 8>([&](auto E) { v[E] += dpp<XOR2>(v[E]); });
    __builtin_amdgcn_sched_barrier(0);
    v2u A[8];
    auto build = [&](auto E) { const u32 d = pk(v[E], v[E]); A[E] = v2u{d & m0, d & m1}; };
    build(IC(0));
    v4f lo = {0, 0, 0, 0}, hi = {0, 0, 0, 0};
    sfor<0, 8>([&](auto E) {
      if constexpr (E < 7) build(IC(E + 1));
      __builtin_amdgcn_sched_barrier(0);
      lo = mfma16(A[E], v2u{WO[E][0], WO[E][1]}, lo); hi = mfma16(A[E], v2u{WO[E][2], WO[E][3]}, hi);
      __builtin_amdgcn_sched_barrier(0);
    });
    *(v4f*)(lds + lrs) = lo; *(v4f*)(lds + lrs + 16) = hi;
    wg_barrier();
    if (w == 0) {
      __builtin_amdgcn_raw_buffer_store_b128(enc4(sum4(0), T), rW, oss, 0, AUX_SC1);
      if (fin) {
        __builtin_amdgcn_s_sleep(S2);
        const float o = xv + rbf(rbf(final_sum<0>(rW, ofin, WS_S, T)) * gate);
        const auto p2 = __builtin_amdgcn_permlane16_swap(asu(o), asu(o), false, false);
        const u32 d2 = pk(asf(p2[0]), asf(p2[1]));
        const auto p4 = __builtin_amdgcn_permlane32_swap(d2, d2, false, false);
        __builtin_amdgcn_raw_buffer_store_b64(v2u{p4[0], p4[1]}, mk(out, 2 * D_ * 2), oout, 0, AUX_SC1);
        __builtin_amdgcn_raw_buffer_store_b32(T, rW, oep, 0, AUX_SC1);
      }
    }
  }
}

extern "C" void kernel_launch(void* const* d_in, const int* in_sizes, int n_in, void* d_out, int out_size, void* d_ws, size_t ws_size,
                              hipStream_t stream) {
  fused_decode<<<256, 512, 0, stream>>>((const u16*)d_in[0], (const u16*)d_in[1], (const u16*)d_in[2], (const u16*)d_in[3], (const u16*)d_in[4],
                                        (const u16*)d_in[6], (const u16*)d_in[7], (const u16*)d_in[8], (u16*)d_out, (unsigned char*)d_ws);
}
```

```cpp
#include <hip/hip_runtime.h>
#include <stdint.h>
#include <type_traits>

typedef unsigned u32;
typedef unsigned long long u64;
typedef unsigned short u16;
typedef u32 v4u __attribute__((ext_vector_type(4)));
typedef u32 v2u __attribute__((ext_vector_type(2)));
typedef float v4f __attribute__((ext_vector_type(4)));
typedef short s4 __attribute__((ext_vector_type(4)));
typedef __bf16 bf8 __attribute__((ext_vector_type(8)));
typedef __bf16 bf2 __attribute__((ext_vector_type(2)));
typedef __amdgpu_buffer_rsrc_t rsrc_t;

constexpr int D_ = 640, H_ = 32, SK = 4096, DH = 192, F_ = 1536, HD = H_ * DH;
constexpr int AUX_NT = 2, AUX_SC1 = 16;
constexpr u32 OOB = 0x80000000u;
constexpr u32 SENT = 0xFFFFFFFFu;
constexpr int SPIN_CAP = 1 << 18;

constexpr int WS_E = 0;
constexpr int REC = 896, R_O = 256, R_ML = R_O + 768;
constexpr int WS_Q = R_O + 256 * REC;
constexpr int WS_F = WS_Q + 64 * 1536;
constexpr int WS_Y = WS_F + 48 * 1024;
constexpr int WS_S = WS_Y + 240 * 512;
constexpr int WS_P = WS_S + 240 * 512, PSTR = 32;
constexpr int WS_END = WS_P + 16 * 16 * PSTR;

constexpr int NPRE = 14, WMIN = 6;
constexpr int QT = 410, FT = 1050;
constexpr int GSTEP = 49;
constexpr int TAU = 665, XV = 12;
constexpr int TOFF = 190;
constexpr int N0 = 6, CC = 40;
constexpr int ROT = 7;
constexpr int WOT = 55;
constexpr int S0 = 5, S1 = 10, TG = 135;
constexpr int S2 = 8;
constexpr int CK0 = 4, CKS = 4, NCK = 14;
constexpr int CKD = 7, RLEAD = 3;
constexpr int C0 = 2, LATE = 90, MISS = 130;
constexpr int KEEP1 = 28, LMAX = 46, USL = 8, KEEP = 18;
constexpr int GK12 = 68, GK16 = 40, GK = 30, GV = 18, LV = 26, GROW = 3, REST = 5, SIG = 23, RAMP = 16;

#define IC(n) std::integral_constant<int, (n)>{}

constexpr int L_XN = 0;
constexpr int L_Q = L_XN + 4 * 2560;
constexpr int L_H = L_Q + 384;
constexpr int L_ML = L_H + 256;
constexpr int L_FQ = L_ML + 32, L_FP = L_ML + 36, L_CNT = L_ML + 40;
constexpr int L_TB = L_ML + 64;
constexpr int L_RO = L_TB + 256;
constexpr int L_RS = L_RO + 8 * 768;
constexpr int L_SP = L_RS + 9 * 1024;
constexpr int L_END = L_SP + 2048;

template <int I, int N, class F> __device__ __forceinline__ void sfor(F&& f) {
  if constexpr (I < N) { f(std::integral_constant<int, I>{}); sfor<I + 1, N>(f); }
}
#define PIN() do { asm volatile("" ::: "memory"); __builtin_amdgcn_sched_barrier(0); } while (0)
#define SPIN _Pragma("unroll 1") for (int spin = 0; spin < SPIN_CAP; ++spin)
__device__ __forceinline__ void wg_barrier() {
  __builtin_amdgcn_sched_barrier(0);
  asm volatile("s_waitcnt lgkmcnt(0)\n\ts_barrier" ::: "memory");
  __builtin_amdgcn_sched_barrier(0);
}
__device__ __forceinline__ rsrc_t mk(const void* p, u32 bytes) { return __builtin_amdgcn_make_buffer_rsrc((void*)p, 0, bytes, 0x00020000); }
__device__ __forceinline__ float asf(u32 x) { return __builtin_bit_cast(float, x); }
__device__ __forceinline__ u32 asu(float x) { return __builtin_bit_cast(u32, x); }
__device__ __forceinline__ u32 pk(float lo, float hi) { bf2 v = {(__bf16)lo, (__bf16)hi}; return __builtin_bit_cast(u32, v); }
__device__ __forceinline__ float rbf(float x) { return (float)(__bf16)x; }
__device__ __forceinline__ float lo16(u32 x) { return asf(x << 16); }
__device__ __forceinline__ float hi16(u32 x) { return asf(x & 0xffff0000u); }
template <int C> __device__ __forceinline__ float dpp(float x) { return asf(__builtin_amdgcn_mov_dpp(asu(x), C, 0xf, 0xf, true)); }
constexpr int XOR1 = 0xB1, XOR2 = 0x4E, HMIR = 0x141, ROR8 = 0x128;
__device__ __forceinline__ float sum16(float x) { auto s = __builtin_amdgcn_permlane16_swap(asu(x), asu(x), false, false); return asf(s[0]) + asf(s[1]); }
__device__ __forceinline__ float sum32(float x) { auto s = __builtin_amdgcn_permlane32_swap(asu(x), asu(x), false, false); return asf(s[0]) + asf(s[1]); }
__device__ __forceinline__ float max16(float x) { auto s = __builtin_amdgcn_permlane16_swap(asu(x), asu(x), false, false); return fmaxf(asf(s[0]), asf(s[1])); }
__device__ __forceinline__ float max32(float x) { auto s = __builtin_amdgcn_permlane32_swap(asu(x), asu(x), false, false); return fmaxf(asf(s[0]), asf(s[1])); }
__device__ __forceinline__ float sum8(float x) { x += dpp<XOR1>(x); x += dpp<XOR2>(x); x += dpp<HMIR>(x); return x; }
__device__ __forceinline__ float half_sum(float x) { x = sum8(x); x += dpp<ROR8>(x); return sum16(x); }
__device__ __forceinline__ float dot2(u32 a, u32 b, float c) { return __builtin_amdgcn_fdot2_f32_bf16(__builtin_bit_cast(bf2, a), __builtin_bit_cast(bf2, b), c, false); }
__device__ __forceinline__ v4f mfma32(v4u a, v4u b, v4f c) { return __builtin_amdgcn_mfma_f32_16x16x32_bf16(__builtin_bit_cast(bf8, a), __builtin_bit_cast(bf8, b), c, 0, 0, 0); }
__device__ __forceinline__ v4f mfma16(v2u a, v2u b, v4f c) { return __builtin_amdgcn_mfma_f32_16x16x16bf16_1k(__builtin_bit_cast(s4, a), __builtin_bit_cast(s4, b), c, 0, 0, 0); }
template <int N> __device__ __forceinline__ u32 dup(u32 x) { return __builtin_amdgcn_perm(x, x, (N & 1) ? 0x03020302u : 0x01000100u); }
__device__ __forceinline__ u32 nosent(float x) { u32 u = asu(x); return u < 0xFFFFFFFEu ? u : 0xFFFFFFFEu; }
__device__ __forceinline__ float ex2(float x) { return __builtin_amdgcn_exp2f(x); }
constexpr float LOG2E = 1.4426950408889634f;

__device__ __forceinline__ v4u enc4(v4f x, u32 T) {
  return v4u{__builtin_amdgcn_perm(asu(x[0]), T, 0x07060500u), __builtin_amdgcn_perm(asu(x[1]), T, 0x07060501u),
             __builtin_amdgcn_perm(asu(x[2]), T, 0x07060502u), __builtin_amdgcn_perm(asu(x[3]), T, 0x07060503u)};
}
__device__ __forceinline__ u32 tag4(v4u x) { return __builtin_amdgcn_perm(x[1], x[0], 0x0c0c0400u) | __builtin_amdgcn_perm(x[3], x[2], 0x04000c0cu); }
__device__ __forceinline__ u32 tagdiff(v4u x, u32 T) { return __builtin_amdgcn_sad_u8(tag4(x), T, 0u); }
__device__ __forceinline__ float dec(u32 x) { return asf(x & 0xffffff00u); }
__device__ __forceinline__ v4u nibbles(u32 T, int odd) { const u32 t = T >> (16 * odd); return v4u{t & 15u, (t >> 4) & 15u, (t >> 8) & 15u, (t >> 12) & 15u}; }
__device__ __forceinline__ v4u enc4n(v4f x, u32 T, int odd) { return (__builtin_bit_cast(v4u, x) & 0xfffffff0u) | nibbles(T, odd); }
template <int STRIDE, int LANES> __device__ __forceinline__ v4f gather2(rsrc_t rW, u32 off, int l, u32 T) {
  v4u a, b;
  const v4u nb = nibbles(T, l & 1);
  SPIN {
    asm volatile("" ::: "memory");
    a = __builtin_amdgcn_raw_buffer_load_b128(rW, l < LANES ? off : OOB, 0, AUX_SC1);
    b = __builtin_amdgcn_raw_buffer_load_b128(rW, l < LANES ? off : OOB, STRIDE, AUX_SC1);
    const v4u m = (a ^ nb) | (b ^ nb);
    if (__all(l >= LANES || ((m[0] | m[1] | m[2] | m[3]) & 15u) == 0u)) break;
    __builtin_amdgcn_s_sleep(16);
  }
  return __builtin_bit_cast(v4f, a & 0xfffffff0u) + __builtin_bit_cast(v4f, b & 0xfffffff0u);
}
template <int SLEEP> __device__ __forceinline__ float final_sum(rsrc_t rW, u32 off, int base, u32 T) {
  v4u t[3];
  SPIN {
    asm volatile("" ::: "memory");
    sfor<0, 3>([&](auto I) { t[I] = __builtin_amdgcn_raw_buffer_load_b128(rW, off, base + I * 8 * 512, AUX_SC1); });
    if (__builtin_amdgcn_ballot_w64(tagdiff(t[0], T) + tagdiff(t[1], T) + tagdiff(t[2], T) != 0u) == 0ull) break;
    if (SLEEP) __builtin_amdgcn_s_sleep(SLEEP);
  }
  const v4f s = __builtin_bit_cast(v4f, t[0] & 0xffffff00u) + __builtin_bit_cast(v4f, t[1] & 0xffffff00u) + __builtin_bit_cast(v4f, t[2] & 0xffffff00u);
  const auto a = __builtin_amdgcn_permlane32_swap(asu(s[0]), asu(s[2]), false, false), b = __builtin_amdgcn_permlane32_swap(asu(s[1]), asu(s[3]), false, false);
  const auto d = __builtin_amdgcn_permlane16_swap(asu(asf(a[0]) + asf(a[1])), asu(asf(b[0]) + asf(b[1])), false, false);
  const float v = asf(d[0]) + asf(d[1]);
  return v + dpp<ROR8>(v);
}
constexpr int win_of(int u) { return NPRE - u / 3 > WMIN ? NPRE - u / 3 : WMIN; }
constexpr int issued_before(int u) { return u + win_of(u) < 64 ? u + win_of(u) : 64; }
extern "C" __device__ u32 raw_load_b32(v4u, u32, u32, u32) __asm("llvm.amdgcn.raw.buffer.load.i32");
extern "C" __device__ u64 dispatch_id() __asm("llvm.amdgcn.dispatch.id");
__device__ __forceinline__ u32 clk() { return (u32)__builtin_amdgcn_s_memrealtime(); }
__device__ __forceinline__ u32 vm_out() { return __builtin_amdgcn_s_getreg(7 | (3 << 11)); }
template <class NN = std::integral_constant<int, N0>> __device__ __forceinline__ void pace(u32 lim, u32& now, NN = NN{}) {
  u32 a;
  asm volatile("1:\n\ts_getreg_b32 %0, hwreg(HW_REG_IB_STS, 0, 4)\n\ts_max_u32 %0, %0, %3\n\ts_mulk_i32 %0, %4\n\ts_add_u32 %0, %0, %2\n\t"
               "s_sub_u32 %0, %1, %0\n\ts_cmp_ge_i32 %0, 0\n\ts_cbranch_scc1 2f\n\t"
               "s_memrealtime vcc\n\ts_waitcnt lgkmcnt(0)\n\ts_mov_b32 %1, vcc_lo\n\ts_branch 1b\n2:"
               : "=&s"(a), "+s"(now) : "s"(lim), "n"(NN::value), "n"(CC) : "vcc", "scc", "memory");
}

__global__ void __launch_bounds__(512, 2)
fused_decode(const u16* x, const u16* kc, const u16* vc, const u16* wq, const u16* wo, const u16* wg, const u16* wu, const u16* wd,
             u16* out, unsigned char* ws) {
  __shared__ __attribute__((aligned(16))) unsigned char lds[L_END];
  u64 pc; u32 clen;
  asm volatile("s_getpc_b64 %0\n.Lcode_lo:\n\ts_mov_b32 %1, .Lcode_hi-.Lcode_lo" : "=s"(pc), "=s"(clen));
  const u32 cpf = raw_load_b32(v4u{(u32)pc, (u32)(pc >> 32) & 0xffffu, clen, 0x00020000u}, threadIdx.x * 2048 + blockIdx.x * 8, 0, 0);
  PIN();
  const int tid = threadIdx.x, l = tid & 63, w = __builtin_amdgcn_readfirstlane(tid >> 6);
  const int bid = blockIdx.x, xg = bid & 7, idx = bid >> 3;
  const int r = l >> 3, c = l & 7, g = l >> 4, col = l & 15;
  const bool pf = idx >= 26;
  const int ks = idx & 1, pu = idx >> 1, pcol = pu * 8 + xg - (pf ? 104 : 0);
  const u32 pstr = pf ? F_ * 2 : HD * 2;
  const u32 op = (u32)(r * 10 + 320 * ks + 80 * w) * pstr + (u32)(pcol * 128 + c * 16) + (pu == 12 ? OOB : 0u);
  const u32 oxl = (u32)(tid & 0x13f) * 40u;
  u32 vzero;
  asm("v_mov_b32 %0, 0" : "=v"(vzero));
  asm volatile("" :: "s"(x), "s"(kc), "s"(vc), "s"(wq), "s"(wo), "s"(wg), "s"(wu), "s"(wd), "s"(out), "s"(ws), "v"(op), "v"(oxl));
  const rsrc_t rX = mk(x, 2 * D_ * 2);
  const rsrc_t rW = mk(ws, WS_END);
  const v4u xa = __builtin_amdgcn_raw_buffer_load_b128(rX, oxl, 0, 0);
  const v4u xb = __builtin_amdgcn_raw_buffer_load_b128(rX, oxl + 16, 0, 0);
  const v2u xc = __builtin_amdgcn_raw_buffer_load_b64(rX, oxl + 32, 0, 0);
  const u32 e = __builtin_amdgcn_raw_buffer_load_b32(rW, vzero, WS_E, AUX_SC1);
  PIN();
  int h, b, sp, G, lc;
  bool tile;
  auto setup = [&]() {
    int i2 = idx;
    asm volatile("" : "+s"(i2));
    h = ((i2 >> 2) & 3) * 8 + xg; b = i2 >> 4; sp = i2 & 3;
    tile = i2 < 30; G = xg * 3 + i2 / 10; lc = i2 % 10;
  };
  auto tag_of = [&](u32 ep) { return ep ^ (((u32)dispatch_id() * 0x9E3779B1u) | 0x11111111u); };
  auto wait_flag = [&](int off, auto SL) {
    _Pragma("unroll 1") for (int spin = 0; spin < SPIN_CAP * 64 / (SL + 2); ++spin) {
      asm volatile("" ::: "memory");
      if (__builtin_amdgcn_readfirstlane(*(const u32*)(lds + off)) != SENT) break;
      __builtin_amdgcn_s_sleep(SL.value);
    }
    asm volatile("" ::: "memory");
  };

  v4u WO[4];
  auto issWO = [&](bool on) {
    const u32 o = (u32)((256 * G + 32 * w + 4 * r) * (D_ * 2) + lc * 128 + c * 16) | (tile && on ? 0u : OOB);
    sfor<0, 4>([&](auto N) { WO[N] = __builtin_amdgcn_raw_buffer_load_b128(mk(wo, HD * D_ * 2), o, N * (D_ * 2), AUX_NT); });
    PIN();
  };
  auto line_end = [&]() { asm volatile("" ::: "memory"); return __builtin_amdgcn_readfirstlane(*(const u32*)(lds + L_TB + 63 * 4)) + (TOFF + 63 * TAU / 16); };
  u32 m0, m1, oo, lrs;
  auto hop1_setup = [&]() {
    const u32 hm = (l & 1) ? 0xffff0000u : 0x0000ffffu;
    m0 = (l & 2) ? 0u : hm; m1 = (l & 2) ? hm : 0u;
    const int R0 = 256 * G + 32 * w, hh = R0 / DH;
    oo = (u32)((hh * 8 + (col >> 3) * 4 + (l & 3)) * REC + R_O + (R0 - hh * DH + 4 * (2 * g + ((col >> 2) & 1))) * 4);
    lrs = (g & 1) == (col >> 3) ? (u32)(L_RS + w * 1024 + (g & 1) * 512 + (g >> 1) * 256 + c * 32) : (u32)(L_SP + l * 32);
    asm volatile("" : "+v"(m0), "+v"(m1), "+v"(oo), "+v"(lrs));
  };
  auto hop1 = [&](u32 T, u32 tgate) {
    while ((int)(clk() - tgate) < 0) {}
    v4u o4;
    SPIN {
      asm volatile("" ::: "memory");
      o4 = __builtin_amdgcn_raw_buffer_load_b128(rW, oo, 0, AUX_SC1);
      if (__builtin_amdgcn_ballot_w64(tag4(o4) != T) == 0ull) break;
    }
    float v[4];
    sfor<0, 4>([&](auto E) { v[E] = dec(o4[E.value]); });
    __builtin_amdgcn_sched_barrier(0);
    sfor<0, 4>([&](auto E) { v[E] += dpp<XOR1>(v[E]); });
    __builtin_amdgcn_sched_barrier(0);
    sfor<0, 4>([&](auto E) { v[E] += dpp<XOR2>(v[E]); });
    __builtin_amdgcn_sched_barrier(0);
    v2u A[4];
    sfor<0, 4>([&](auto E) { const u32 d = pk(v[E], v[E]); A[E] = v2u{d & m0, d & m1}; });
    __builtin_amdgcn_sched_barrier(0);
    v4f lo = {0, 0, 0, 0}, hi = {0, 0, 0, 0};
    sfor<0, 4>([&](auto E) { lo = mfma16(A[E], v2u{WO[E][0], WO[E][1]}, lo); hi = mfma16(A[E], v2u{WO[E][2], WO[E][3]}, hi); });
    *(v4f*)(lds + lrs) = lo; *(v4f*)(lds + lrs + 16) = hi;
    wg_barrier();
  };

  if (w >= 4) {
    const int sw = w - 4;
    wg_barrier();
    setup();
    const int bh = b * H_ + h;
    const u32 okv = (u32)((sp * 32 + sw * 8 + r) * 384 + c * 16);
    auto kvstep = [&](int i) { return (u32)(((i + bh * ROT) & 31) * 49152); };
    const rsrc_t rK = mk((const unsigned char*)kc + (u32)(bh * (SK * DH * 2)), SK * DH * 2);
    const rsrc_t rV = mk((const unsigned char*)vc + (u32)(bh * (SK * DH * 2)), SK * DH * 2);
    v4u KV[64][3];
    u32 zt[3];
    {
      const u32 o3 = (u32)(l * 524288 - (l == 3 ? 4 : 0)) | (l < 4 ? 0u : OOB);
      zt[0] = __builtin_amdgcn_raw_buffer_load_b32(rK, o3 | (sw == 0 ? 0u : OOB), 0, AUX_NT);
      zt[1] = __builtin_amdgcn_raw_buffer_load_b32(rV, o3 | (sw == 1 ? 0u : OOB), 0, AUX_NT);
      zt[2] = __builtin_amdgcn_raw_buffer_load_b32(mk(wo, HD * D_ * 2), (u32)((256 * G + 255 * l) * (D_ * 2)) | ((l < 2 && sw == 2) ? 0u : OOB), 0, AUX_NT);
      PIN();
    }
    const int i0 = ((l + 1 - b) & ~1) + b - 1, ph = (bh >> 1) & 15;
    const int tp = TOFF + ((i0 * 16 + (i0 < 63 ? ph : ph * 3 / 4 - 5)) * TAU >> 8);
    const int tl = tp - N0 * CC, tm = tp + (C0 + 3) * (TAU / 48), otb = (i0 > 0 ? i0 : 0) * 4;
    u32 tb = *(const u32*)(lds + L_TB + l * 4);
    u32 tnow = clk();
    const u32 orep = l == 0 ? (u32)((b * 8 + xg) * PSTR) : OOB;
    const int rep = sw == 0 && idx == 5 ? 0 : sw == 0 && idx == 22 ? 1 : -1;
    auto iss = [&](auto I) {
      pace(__builtin_amdgcn_readlane(tb + tl, I), tnow);
      constexpr int J = (I + 1) & ~1;
      if constexpr (J >= CK0 && (J - CK0) % CKS == 0 && (J - CK0) / CKS < NCK) {
        if (rep == (I & 1)) {
          const u32 now = clk(), v = now + vm_out() * (TAU / 48) - __builtin_amdgcn_readlane(tm, I);
          if ((int)(now - __builtin_amdgcn_readlane(tb, I)) < TOFF + (J + CKD - RLEAD) * TAU / 16 - LATE)
            __builtin_amdgcn_raw_buffer_store_b64(v2u{v, tag_of(e)}, rW, orep, WS_P + (J - CK0) / CKS * 16 * PSTR, AUX_SC1);
        }
      }
      sfor<0, 3>([&](auto P) { KV[I][P] = __builtin_amdgcn_raw_buffer_load_b128(I < 32 ? rK : rV, okv + P * 128, kvstep(I), AUX_NT); });
      if constexpr (I >= CK0 + CKD - 2) tb = *(const u32*)(lds + L_TB + otb);
      PIN();
    };
    sfor<0, NPRE>(iss);
    asm volatile("" :: "v"(zt[0]), "v"(zt[1]), "v"(zt[2]), "v"(cpf), "v"(e));
    wait_flag(L_FQ, IC(1));
    v4u q[3];
    sfor<0, 3>([&](auto P) { q[P] = *(const v4u*)(lds + L_Q + P * 128 + c * 16); });
    PIN();
    float sc[32], mw;
    sfor<0, 32>([&](auto I) {
      float d0 = 0.f, d1 = 0.f, d2 = 0.f;
      sfor<0, 4>([&](auto E) { d0 = dot2(KV[I][0][E.value], q[0][E.value], d0); d1 = dot2(KV[I][1][E.value], q[1][E.value], d1); d2 = dot2(KV[I][2][E.value], q[2][E.value], d2); });
      sc[I] = rbf(rbf(sum8(d0 + d1 + d2)) * 0.07216878364870322f);
      mw = I == 0 ? sc[I] : fmaxf(mw, sc[I]);
      asm volatile("" : "+v"(sc[I]), "+v"(mw));
      PIN();
      sfor<issued_before(I), issued_before(I + 1)>(iss);
    });
    mw = fmaxf(mw, dpp<ROR8>(mw)); mw = max32(max16(mw));
    v4u msk;
    sfor<0, 4>([&](auto I) { msk[I.value] = (c >> 1) == I ? ((c & 1) ? 0xffff0000u : 0x0000ffffu) : 0u; });
    float lw = 0.f;
    u32 pp[32], Tq, lro, tgate;
    v4f acc[3] = {{0, 0, 0, 0}, {0, 0, 0, 0}, {0, 0, 0, 0}};
    sfor<32, 64>([&](auto I) {
      if constexpr (I < 40) {
        sfor<(I - 32) * 4, (I - 31) * 4>([&](auto N) { const float p = ex2((sc[N] - mw) * LOG2E); lw += p; pp[N] = pk(p, p); asm volatile("" : "+v"(pp[N])); });
        asm volatile("" : "+v"(lw));
      }
      if constexpr (I == 39) {
        lw += dpp<ROR8>(lw); lw = sum32(sum16(lw));
        *(v2u*)(lds + L_ML + sw * 8) = v2u{nosent(mw), nosent(lw)};
      }
      const v4u a = msk & pp[I - 32];
      sfor<0, 3>([&](auto P) { acc[P] = mfma32(a, KV[I][P], acc[P]); });
      if constexpr (I < 63) asm volatile("" : "+v"(acc[0]), "+v"(acc[1]), "+v"(acc[2]));
      PIN();
      sfor<issued_before(I), issued_before(I + 1)>(iss);
      if constexpr (I == GSTEP) { *(u32*)(lds + L_FP) = 1u; PIN(); }
      if constexpr (I == 50) {
        hop1_setup();
        Tq = tag_of(e);
        lro = (g >> 1) == (col >> 3) ? (u32)(L_RO + ((2 * sw + (g >> 1)) * 192 + c * 8 + 4 * (g & 1)) * 4) : (u32)(L_SP + l * 16);
        asm volatile("" : "+v"(Tq), "+v"(lro));
        PIN();
      }
      if constexpr (I == 60) {
        pace(line_end() + WOT - 15 * CC, tnow, IC(15));
        tgate = line_end() + TG;
        issWO(true);
      }
    });
    sfor<0, 3>([&](auto P) { *(v4f*)(lds + lro + P * 256) = acc[P]; });
    wg_barrier();
    if (tile) { __builtin_amdgcn_s_sleep(S1); hop1(Tq, tgate); }
    asm volatile(".Lcode_hi:");
    return;
  }

  v4u P0[10], P1[10], F2[8];
  {
    const rsrc_t rP = mk(pf ? wg : wq, D_ * pstr);
    sfor<0, 10>([&](auto I) { P0[I] = __builtin_amdgcn_raw_buffer_load_b128(rP, op, I * pstr, AUX_NT); __builtin_amdgcn_sched_barrier(0); });
    PIN();
  }
  setup();
  if (tid < 3) *(v4u*)(lds + L_ML + l * 16) = v4u{SENT, SENT, SENT, SENT};
  if (w == 1) { asm volatile("" :: "v"(e)); PIN(); *(u32*)(lds + L_TB + l * 4) = clk(); }
  wg_barrier();
  issWO(!pf);
  if (pf) sfor<0, 10>([&](auto I) { P1[I] = __builtin_amdgcn_raw_buffer_load_b128(mk(wu, D_ * F_ * 2), op, I * (F_ * 2), AUX_NT); });
  PIN();
  const u32 T = tag_of(e);
  {
    const u32 xd[10] = {xa[0], xa[1], xa[2], xa[3], xb[0], xb[1], xb[2], xb[3], xc[0], xc[1]};
    float s = 0.f;
    sfor<0, 10>([&](auto I) { s += lo16(xd[I]) + hi16(xd[I]); });
    const float mean = half_sum(s) * (1.f / D_);
    float v = 0.f;
    sfor<0, 10>([&](auto I) { const float d0 = lo16(xd[I]) - mean, d1 = hi16(xd[I]) - mean; v += d0 * d0 + d1 * d1; });
    const float var = half_sum(v) * (1.f / D_);
    const float mb = rbf(mean), inv = __builtin_amdgcn_rcpf(rbf(__builtin_sqrtf(rbf(rbf(var) + 1e-5f))));
    sfor<0, 5>([&](auto I) {
      const u32 d0 = pk(lo16(xd[2 * I]) - mb, hi16(xd[2 * I]) - mb), d1 = pk(lo16(xd[2 * I + 1]) - mb, hi16(xd[2 * I + 1]) - mb);
      *(v2u*)(lds + L_XN + w * 2560 + l * 40 + I * 8) = v2u{pk(lo16(d0) * inv, hi16(d0) * inv), pk(lo16(d1) * inv, hi16(d1) * inv)};
    });
    PIN();
  }
  sfor<0, 8>([&](auto I) { F2[I] = __builtin_amdgcn_raw_buffer_load_b128(mk(wd, F_ * D_ * 2), (u32)((64 * G + 8 * r) * (D_ * 2) + lc * 128 + c * 16) | ((w == 2 && tile) ? 0u : OOB), I * (D_ * 2), AUX_NT); });
  PIN();
  const u32 t0 = __builtin_amdgcn_readfirstlane(*(const u32*)(lds + L_TB));
  auto wait_clock = [&](int ticks) { while ((int)(clk() - t0) < ticks) __builtin_amdgcn_s_sleep(2); };
  const int ab = col >> 3, ar = 2 * g + ((col >> 2) & 1);
  hop1_setup();
  auto gemv = [&](u32 d, v4u W, v4f& lo, v4f& hi) { const v2u A = {d & m0, d & m1}; lo = mfma16(A, v2u{W[0], W[1]}, lo); hi = mfma16(A, v2u{W[2], W[3]}, hi); };
  const bool dv = (g & 1) == (col >> 3);
  const u32 ld0 = (u32)((g & 1) * 512 + (g >> 1) * 256 + c * 32);
  auto put = [&](int slot, v4f lo, v4f hi) { if (dv) { *(v4f*)(lds + L_RS + slot * 1024 + ld0) = lo; *(v4f*)(lds + L_RS + slot * 1024 + ld0 + 16) = hi; } };
  auto sum4 = [&](int slot) {
    v4f t[4];
    sfor<0, 4>([&](auto I) { t[I] = *(const v4f*)(lds + L_RS + (slot + I) * 1024 + l * 16); });
    v4f s = (t[0] + t[1]) + (t[2] + t[3]);
    sfor<0, 4>([&](auto E) { s[E.value] = sum32(s[E.value]); });
    return s;
  };
  {
    u32 xv[5];
    sfor<0, 5>([&](auto I) { xv[I] = *(const u32*)(lds + L_XN + w * 2560 + ab * 1280 + (320 * ks + 80 * w + 10 * ar) * 2 + I * 4); });
    v4f lo = {0, 0, 0, 0}, hi = {0, 0, 0, 0};
    sfor<0, 10>([&](auto I) { gemv(dup<I>(xv[I / 2]), P0[I], lo, hi); });
    put(w, lo, hi);
    if (pf) {
      lo = v4f{0, 0, 0, 0}; hi = v4f{0, 0, 0, 0};
      sfor<0, 10>([&](auto I) { gemv(dup<I>(xv[I / 2]), P1[I], lo, hi); });
      put(4 + w, lo, hi);
    }
    asm volatile("" ::: "memory");
    u32 n = 0;
    if (l == 0) n = __hip_atomic_fetch_add((u32*)(lds + L_CNT), 1u, __ATOMIC_RELAXED, __HIP_MEMORY_SCOPE_WORKGROUP);
    if (__builtin_amdgcn_readfirstlane(n) == SENT + 3u) {
      asm volatile("" ::: "memory");
      const u32 o = pf ? (u32)(WS_F + (pcol * 2 + ks) * 1024 + l * 16)
                       : (u32)(WS_Q + ks * 49152 + (l >> 4) * 24576 + pcol * 256 + (l & 15) * 16);
      const u32 om = o | ((l < 32 && pu != 12) ? 0u : OOB);
      __builtin_amdgcn_raw_buffer_store_b128(enc4n(sum4(0), T, l & 1), rW, om, 0, AUX_SC1);
      if (pf) __builtin_amdgcn_raw_buffer_store_b128(enc4n(sum4(4), T, l & 1), rW, om, 512, AUX_SC1);
    }
    PIN();
  }
  if (pf) issWO(true);
  const int me = h * 8 + b * 4 + sp;
  const bool fin = w == 0 && bid < 40;
  const int fq = (bid & 3) * 8 + c;
  const u32 ofin = (u32)(((bid >> 2) * 24 + r) * 512 + fq * 16);
  const u32 oidx = (u32)(((fq >> 4) * D_ + (bid >> 2) * 64 + (fq & 15) * 4) * 2);
  u32 oout = (fin && l < 8) ? oidx : OOB;
  v4f fw = {0, 0, 0, 0};
  float gate = 0.f, xv = 0.f;

  if (w == 3) {
    wait_clock(QT);
    const v4f s = gather2<49152, 48>(rW, (u32)(WS_Q + b * 24576 + h * 768 + l * 16), l, T);
    if (l < 48) *(v2u*)(lds + L_Q + l * 8) = v2u{pk(s[0], s[1]), pk(s[2], s[3])};
    asm volatile("" ::: "memory");
    if (l == 0) *(u32*)(lds + L_FQ) = 1u;
  } else if (w == 2) {
    if (tile) {
      wait_clock(FT);
      const v4f s = gather2<1024, 64>(rW, (u32)(WS_F + G * 2048 + l * 16), l, T);
      float hv[4];
      sfor<0, 4>([&](auto E) {
        const auto t = __builtin_amdgcn_permlane32_swap(asu(s[E.value]), asu(s[E.value]), false, false);
        const float gg = rbf(asf(t[0])), uu = rbf(asf(t[1]));
        const float z = 0.7978845608028654f * (gg + 0.044715f * gg * gg * gg);
        hv[E] = rbf(gg / (1.f + ex2(-2.f * LOG2E * z))) * uu;
      });
      if (l < 32) *(v2u*)(lds + L_H + l * 8) = v2u{pk(hv[0], hv[1]), pk(hv[2], hv[3])};
      const v4u hh = *(const v4u*)(lds + L_H + ab * 128 + ar * 16);
      v4f lo = {0, 0, 0, 0}, hi = {0, 0, 0, 0};
      sfor<0, 8>([&](auto I) { gemv(dup<I>(hh[I / 2]), F2[I], lo, hi); });
      put(8, lo, hi);
      v4f y = *(const v4f*)(lds + L_RS + 8 * 1024 + l * 16);
      sfor<0, 4>([&](auto E) { y[E.value] = sum32(y[E.value]); });
      __builtin_amdgcn_raw_buffer_store_b128(enc4(y, T), rW, l < 32 ? (u32)(WS_Y + (lc * 24 + G) * 512 + l * 16) : OOB, 0, AUX_SC1);
    }
  } else if (w == 1) {
    int base = 0, m1_ = 0, m2_ = 0;
    bool tilted = false;
    _Pragma("unroll 1") for (int k = 0; k < NCK; ++k) {
      const int S = CK0 + k * CKS, E = S + CKD, end = S < 32 ? 32 : 64, bS = __builtin_amdgcn_readlane(base, S);
      wait_clock(__builtin_amdgcn_readlane(base, E - RLEAD) + TOFF + (E - RLEAD) * TAU / 16);
      asm volatile("" ::: "memory");
      const v2u z = __builtin_amdgcn_raw_buffer_load_b64(rW, (u32)(WS_P + (k * 16 + col) * PSTR), 0, AUX_SC1);
      int v = (int)(z[0] - t0);
      if (z[1] != T || (u32)(v + 1000) > 2000u) v = bS + MISS;
      v += __builtin_amdgcn_mov_dpp(v, XOR1, 0xf, 0xf, true); v += __builtin_amdgcn_mov_dpp(v, XOR2, 0xf, 0xf, true);
      v += __builtin_amdgcn_mov_dpp(v, HMIR, 0xf, 0xf, true); v += __builtin_amdgcn_mov_dpp(v, ROR8, 0xf, 0xf, true);
      const int m = __builtin_amdgcn_readfirstlane(v) >> 4;
      if (S == 32) tilted = false;
      if (k == 0) base = l >= E ? m - (l > 32 ? (l - 32) * XV / 16 : 0) : 0;
      else {
        const int bl = m - __builtin_amdgcn_readlane(base, E + 3 < 63 ? E + 3 : 63);
        int d = 0;
        if (k == 1) d = bl - KEEP1;
        else if (!tilted && (S < 32 || S >= 40) && m - m2_ >= (S == 12 ? GK12 : S == 16 ? GK16 : S < 32 ? GK : GV) && m - bS >= LV) {
          d = bl + GROW * (CKD < end - S ? CKD : end - S) - REST;
          const int n2 = (l < end ? l : end) - E;
          base += n2 > 0 ? n2 * (2 * SIG + (S < 32 ? 0 : XV)) / 16 : 0;
          tilted = true;
        } else if (bl > LMAX - (S > 32 ? (S - 32) * USL / 16 : 0)) d = bl - KEEP;
        const int rp = RAMP * (l - (E - 1));
        if (d > 0) base += rp < 0 ? 0 : rp < d ? rp : d;
      }
      m2_ = m1_; m1_ = m;
      *(u32*)(lds + L_TB + l * 4) = t0 + base;
    }
  } else {
    wait_flag(L_FQ, IC(16));
    xv = lo16(__builtin_amdgcn_raw_buffer_load_b16(rX, fin ? oidx + g * 2 : OOB, 0, 0));
    {
      const u32 z = __builtin_amdgcn_raw_buffer_load_b32(rW, l < 6 ? (u32)(me * REC + R_O + l * 128) : (l < 10 && tile) ? (u32)(WS_S + (lc * 24 + G) * 512 + (l - 6) * 128) : OOB, 0, AUX_SC1);
      asm volatile("" :: "v"(z));
    }
    __builtin_amdgcn_raw_buffer_store_b64(v2u{0u, 0u}, mk(out, 2 * D_ * 2), oout, 0, AUX_SC1);
    v2u mlw;
    _Pragma("unroll 1") for (int spin = 0; spin < SPIN_CAP * 4; ++spin) {
      asm volatile("" ::: "memory");
      mlw = *(const v2u*)(lds + L_ML + (l & 3) * 8);
      if (__all(mlw[0] != SENT && mlw[1] != SENT)) break;
      __builtin_amdgcn_s_sleep(16);
    }
    float Mg = asf(mlw[0]); Mg = fmaxf(Mg, dpp<XOR1>(Mg)); Mg = fmaxf(Mg, dpp<XOR2>(Mg));
    float Lg = asf(mlw[1]) * ex2((asf(mlw[0]) - Mg) * LOG2E); Lg += dpp<XOR1>(Lg); Lg += dpp<XOR2>(Lg);
    __builtin_amdgcn_raw_buffer_store_b128(enc4(v4f{Mg, Lg, 0.f, 0.f}, T), rW, l < 8 ? (u32)(me * REC + R_ML + l * 16) : OOB, 0, AUX_SC1);
    if (fin) {
      const float y = rbf(final_sum<16>(rW, ofin, WS_Y, T));
      gate = rbf(y / (1.f + ex2(-LOG2E * y)));
    }
    wait_flag(L_FP, IC(16));
    v4u ml4;
    SPIN {
      asm volatile("" ::: "memory");
      ml4 = __builtin_amdgcn_raw_buffer_load_b128(rW, (u32)((h * 8 + b * 4 + (l & 3)) * REC + R_ML + ((l >> 2) & 7) * 16), 0, AUX_SC1);
      if (__all(tag4(ml4) == T)) break;
      __builtin_amdgcn_s_sleep(16);
    }
    float M = dec(ml4[0]); M = fmaxf(M, dpp<XOR1>(M)); M = fmaxf(M, dpp<XOR2>(M));
    float L = dec(ml4[1]) * ex2((dec(ml4[0]) - M) * LOG2E); L += dpp<XOR1>(L); L += dpp<XOR2>(L);
    const float f = ex2((asf(mlw[0]) - M) * LOG2E) / L;
    fw = v4f{dpp<0x00>(f), dpp<0x55>(f), dpp<0xAA>(f), dpp<0xFF>(f)};
  }

  asm volatile("" :: "v"(cpf));
  __builtin_amdgcn_s_waitcnt(0x0F70);
  u32 ost = l < 48 ? (u32)(me * REC + R_O + l * 16) : OOB, oss = l < 32 ? (u32)(WS_S + (lc * 24 + G) * 512 + l * 16) : OOB;
  u32 oep = (bid == 0 && l == 0) ? (u32)WS_E : OOB;
  asm volatile("" : "+v"(ost), "+v"(oss), "+v"(oout), "+v"(oep), "+v"(xv), "+v"(fw), "+v"(gate));
  wg_barrier();
  if (w == 0) {
    v4f t[8];
    sfor<0, 8>([&](auto I) { t[I] = *(const v4f*)(lds + L_RO + I * 768 + l * 16); });
    __builtin_amdgcn_sched_barrier(0);
    const v4f s = ((t[0] + t[1]) * fw[0] + (t[2] + t[3]) * fw[1]) + ((t[4] + t[5]) * fw[2] + (t[6] + t[7]) * fw[3]);
    __builtin_amdgcn_raw_buffer_store_b128(enc4(s, T), rW, ost, 0, AUX_SC1);
  } else {
    __builtin_amdgcn_s_sleep(S1 - S0 - 1);
  }
  if (tile) {
    __builtin_amdgcn_s_sleep(S0);
    hop1(T, line_end() + TG);
    if (w == 0) {
      v4f t[8];
      sfor<0, 8>([&](auto I) { t[I] = *(const v4f*)(lds + L_RS + I * 1024 + l * 16); });
      __builtin_amdgcn_sched_barrier(0);
      v4f s = ((t[0] + t[1]) + (t[2] + t[3])) + ((t[4] + t[5]) + (t[6] + t[7]));
      sfor<0, 4>([&](auto E) { s[E.value] = sum32(s[E.value]); });
      __builtin_amdgcn_raw_buffer_store_b128(enc4(s, T), rW, oss, 0, AUX_SC1);
      if (fin) {
        __builtin_amdgcn_s_sleep(S2);
        const float o = xv + rbf(rbf(final_sum<0>(rW, ofin, WS_S, T)) * gate);
        const auto p2 = __builtin_amdgcn_permlane16_swap(asu(o), asu(o), false, false);
        const u32 d2 = pk(asf(p2[0]), asf(p2[1]));
        const auto p4 = __builtin_amdgcn_permlane32_swap(d2, d2, false, false);
        __builtin_amdgcn_raw_buffer_store_b64(v2u{p4[0], p4[1]}, mk(out, 2 * D_ * 2), oout, 0, AUX_SC1);
        __builtin_amdgcn_raw_buffer_store_b32(T, rW, oep, 0, AUX_SC1);
      }
    }
  }
}

extern "C" void kernel_launch(void* const* d_in, const int* in_sizes, int n_in, void* d_out, int out_size, void* d_ws, size_t ws_size,
                              hipStream_t stream) {
  fused_decode<<<256, 512, 0, stream>>>((const u16*)d_in[0], (const u16*)d_in[1], (const u16*)d_in[2], (const u16*)d_in[3], (const u16*)d_in[4],
                                        (const u16*)d_in[6], (const u16*)d_in[7], (const u16*)d_in[8], (u16*)d_out, (unsigned char*)d_ws);
}
```

```cpp
#include <hip/hip_runtime.h>
#include <stdint.h>
#include <type_traits>

typedef unsigned u32;
typedef unsigned long long u64;
typedef unsigned short u16;
typedef u32 v4u __attribute__((ext_vector_type(4)));
typedef u32 v2u __attribute__((ext_vector_type(2)));
typedef float v4f __attribute__((ext_vector_type(4)));
typedef short s4 __attribute__((ext_vector_type(4)));
typedef __bf16 bf8 __attribute__((ext_vector_type(8)));
typedef __bf16 bf2 __attribute__((ext_vector_type(2)));
typedef __amdgpu_buffer_rsrc_t rsrc_t;

constexpr int D_ = 640, H_ = 32, SK = 4096, DH = 192, F_ = 1536, HD = H_ * DH;
constexpr int AUX_NT = 2, AUX_SC1 = 16;
constexpr u32 OOB = 0x80000000u;
constexpr u32 SENT = 0xFFFFFFFFu;
constexpr int SPIN_CAP = 1 << 18;

constexpr int WS_E = 0;
constexpr int REC = 896, R_O = 256, R_ML = R_O + 768;
constexpr int WS_Q = R_O + 256 * REC;
constexpr int WS_F = WS_Q + 64 * 1536;
constexpr int WS_Y = WS_F + 48 * 1024;
constexpr int WS_S = WS_Y + 240 * 512;
constexpr int WS_P = WS_S + 240 * 512, PSTR = 32;
constexpr int WS_END = WS_P + 16 * 16 * PSTR;

constexpr int NPRE = 14, WMIN = 6;
constexpr int QT = 410, FT = 1050;
constexpr int GSTEP = 44;
constexpr int TAU = 665, XV = 17, XS = 36;
#define TCAPV 72
constexpr u32 TCAP = TCAPV;
constexpr int TOFF = 190;
constexpr int N0 = 6, CC = 40;
constexpr int ROT = 7;
constexpr int WOT = 55;
constexpr int S0 = 5, S1 = 10, TG = 135;
constexpr int S2 = 8;
constexpr int CK0 = 4, CKS = 4, NCK = 14;
constexpr int CKD = 7, RLEAD = 3;
constexpr int C0 = 2, LATE = 90, MISS = 130;
constexpr int KEEP1 = 28, LMAX = 46, USL = 8, KEEP = 18;
constexpr int GK12 = 68, GK16 = 40, GK = 30, GV = 18, LV = 26, GROW = 3, REST = 5, SIG = 23, RAMP = 16;

#define IC(n) std::integral_constant<int, (n)>{}

constexpr int L_XN = 0;
constexpr int L_Q = L_XN + 4 * 2560;
constexpr int L_H = L_Q + 384;
constexpr int L_ML = L_H + 256;
constexpr int L_FQ = L_ML + 32, L_FP = L_ML + 36, L_CNT = L_ML + 40;
constexpr int L_TB = L_ML + 64;
constexpr int L_RO = L_TB + 256;
constexpr int L_RS = L_RO + 8 * 768;
constexpr int L_SP = L_RS + 9 * 1024;
constexpr int L_END = L_SP + 2048;

template <int I, int N, class F> __device__ __forceinline__ void sfor(F&& f) {
  if constexpr (I < N) { f(std::integral_constant<int, I>{}); sfor<I + 1, N>(f); }
}
#define PIN() do { asm volatile("" ::: "memory"); __builtin_amdgcn_sched_barrier(0); } while (0)
#define SPIN _Pragma("unroll 1") for (int spin = 0; spin < SPIN_CAP; ++spin)
__device__ __forceinline__ void wg_barrier() {
  __builtin_amdgcn_sched_barrier(0);
  asm volatile("s_waitcnt lgkmcnt(0)\n\ts_barrier" ::: "memory");
  __builtin_amdgcn_sched_barrier(0);
}
__device__ __forceinline__ rsrc_t mk(const void* p, u32 bytes) { return __builtin_amdgcn_make_buffer_rsrc((void*)p, 0, bytes, 0x00020000); }
__device__ __forceinline__ float asf(u32 x) { return __builtin_bit_cast(float, x); }
__device__ __forceinline__ u32 asu(float x) { return __builtin_bit_cast(u32, x); }
__device__ __forceinline__ u32 pk(float lo, float hi) { bf2 v = {(__bf16)lo, (__bf16)hi}; return __builtin_bit_cast(u32, v); }
__device__ __forceinline__ float rbf(float x) { return (float)(__bf16)x; }
__device__ __forceinline__ float lo16(u32 x) { return asf(x << 16); }
__device__ __forceinline__ float hi16(u32 x) { return asf(x & 0xffff0000u); }
template <int C> __device__ __forceinline__ float dpp(float x) { return asf(__builtin_amdgcn_mov_dpp(asu(x), C, 0xf, 0xf, true)); }
constexpr int XOR1 = 0xB1, XOR2 = 0x4E, HMIR = 0x141, ROR8 = 0x128;
__device__ __forceinline__ float sum16(float x) { auto s = __builtin_amdgcn_permlane16_swap(asu(x), asu(x), false, false); return asf(s[0]) + asf(s[1]); }
__device__ __forceinline__ float sum32(float x) { auto s = __builtin_amdgcn_permlane32_swap(asu(x), asu(x), false, false); return asf(s[0]) + asf(s[1]); }
__device__ __forceinline__ float max16(float x) { auto s = __builtin_amdgcn_permlane16_swap(asu(x), asu(x), false, false); return fmaxf(asf(s[0]), asf(s[1])); }
__device__ __forceinline__ float max32(float x) { auto s = __builtin_amdgcn_permlane32_swap(asu(x), asu(x), false, false); return fmaxf(asf(s[0]), asf(s[1])); }
__device__ __forceinline__ float sum8(float x) { x += dpp<XOR1>(x); x += dpp<XOR2>(x); x += dpp<HMIR>(x); return x; }
__device__ __forceinline__ float half_sum(float x) { x = sum8(x); x += dpp<ROR8>(x); return sum16(x); }
__device__ __forceinline__ float dot2(u32 a, u32 b, float c) { return __builtin_amdgcn_fdot2_f32_bf16(__builtin_bit_cast(bf2, a), __builtin_bit_cast(bf2, b), c, false); }
__device__ __forceinline__ v4f mfma32(v4u a, v4u b, v4f c) { return __builtin_amdgcn_mfma_f32_16x16x32_bf16(__builtin_bit_cast(bf8, a), __builtin_bit_cast(bf8, b), c, 0, 0, 0); }
__device__ __forceinline__ v4f mfma16(v2u a, v2u b, v4f c) { return __builtin_amdgcn_mfma_f32_16x16x16bf16_1k(__builtin_bit_cast(s4, a), __builtin_bit_cast(s4, b), c, 0, 0, 0); }
template <int N> __device__ __forceinline__ u32 dup(u32 x) { return __builtin_amdgcn_perm(x, x, (N & 1) ? 0x03020302u : 0x01000100u); }
__device__ __forceinline__ u32 nosent(float x) { u32 u = asu(x); return u < 0xFFFFFFFEu ? u : 0xFFFFFFFEu; }
__device__ __forceinline__ float ex2(float x) { return __builtin_amdgcn_exp2f(x); }
constexpr float LOG2E = 1.4426950408889634f;

__device__ __forceinline__ v4u enc4(v4f x, u32 T) {
  return v4u{__builtin_amdgcn_perm(asu(x[0]), T, 0x07060500u), __builtin_amdgcn_perm(asu(x[1]), T, 0x07060501u),
             __builtin_amdgcn_perm(asu(x[2]), T, 0x07060502u), __builtin_amdgcn_perm(asu(x[3]), T, 0x07060503u)};
}
__device__ __forceinline__ u32 tag4(v4u x) { return __builtin_amdgcn_perm(x[1], x[0], 0x0c0c0400u) | __builtin_amdgcn_perm(x[3], x[2], 0x04000c0cu); }
__device__ __forceinline__ u32 tagdiff(v4u x, u32 T) { return __builtin_amdgcn_sad_u8(tag4(x), T, 0u); }
__device__ __forceinline__ float dec(u32 x) { return asf(x & 0xffffff00u); }
__device__ __forceinline__ v4u nibbles(u32 T, int odd) { const u32 t = T >> (16 * odd); return v4u{t & 15u, (t >> 4) & 15u, (t >> 8) & 15u, (t >> 12) & 15u}; }
__device__ __forceinline__ v4u enc4n(v4f x, u32 T, int odd) { return (__builtin_bit_cast(v4u, x) & 0xfffffff0u) | nibbles(T, odd); }
template <int STRIDE, int LANES> __device__ __forceinline__ v4f gather2(rsrc_t rW, u32 off, int l, u32 T) {
  v4u a, b;
  const v4u nb = nibbles(T, l & 1);
  SPIN {
    asm volatile("" ::: "memory");
    a = __builtin_amdgcn_raw_buffer_load_b128(rW, l < LANES ? off : OOB, 0, AUX_SC1);
    b = __builtin_amdgcn_raw_buffer_load_b128(rW, l < LANES ? off : OOB, STRIDE, AUX_SC1);
    const v4u m = (a ^ nb) | (b ^ nb);
    if (__all(l >= LANES || ((m[0] | m[1] | m[2] | m[3]) & 15u) == 0u)) break;
    __builtin_amdgcn_s_sleep(16);
  }
  return __builtin_bit_cast(v4f, a & 0xfffffff0u) + __builtin_bit_cast(v4f, b & 0xfffffff0u);
}
template <int SLEEP> __device__ __forceinline__ float final_sum(rsrc_t rW, u32 off, int base, u32 T) {
  v4u t[3];
  SPIN {
    asm volatile("" ::: "memory");
    sfor<0, 3>([&](auto I) { t[I] = __builtin_amdgcn_raw_buffer_load_b128(rW, off, base + I * 8 * 512, AUX_SC1); });
    if (__builtin_amdgcn_ballot_w64(tagdiff(t[0], T) + tagdiff(t[1], T) + tagdiff(t[2], T) != 0u) == 0ull) break;
    if (SLEEP) __builtin_amdgcn_s_sleep(SLEEP);
  }
  const v4f s = __builtin_bit_cast(v4f, t[0] & 0xffffff00u) + __builtin_bit_cast(v4f, t[1] & 0xffffff00u) + __builtin_bit_cast(v4f, t[2] & 0xffffff00u);
  const auto a = __builtin_amdgcn_permlane32_swap(asu(s[0]), asu(s[2]), false, false), b = __builtin_amdgcn_permlane32_swap(asu(s[1]), asu(s[3]), false, false);
  const auto d = __builtin_amdgcn_permlane16_swap(asu(asf(a[0]) + asf(a[1])), asu(asf(b[0]) + asf(b[1])), false, false);
  const float v = asf(d[0]) + asf(d[1]);
  return v + dpp<ROR8>(v);
}
constexpr int win_of(int u) { return NPRE - u / 3 > WMIN ? NPRE - u / 3 : WMIN; }
constexpr int issued_before(int u) { return u + win_of(u) < 64 ? u + win_of(u) : 64; }
extern "C" __device__ u32 raw_load_b32(v4u, u32, u32, u32) __asm("llvm.amdgcn.raw.buffer.load.i32");
extern "C" __device__ u64 dispatch_id() __asm("llvm.amdgcn.dispatch.id");
__device__ __forceinline__ u32 clk() { return (u32)__builtin_amdgcn_s_memrealtime(); }
__device__ __forceinline__ u32 vm_out() { return __builtin_amdgcn_s_getreg(7 | (3 << 11)); }
template <class NN = std::integral_constant<int, N0>> __device__ __forceinline__ void pace(u32 lim, u32& now, NN = NN{}) {
  u32 a;
  asm volatile("1:\n\ts_getreg_b32 %0, hwreg(HW_REG_IB_STS, 0, 4)\n\ts_max_u32 %0, %0, %3\n\ts_mulk_i32 %0, %4\n\ts_add_u32 %0, %0, %2\n\t"
               "s_sub_u32 %0, %1, %0\n\ts_cmp_ge_i32 %0, 0\n\ts_cbranch_scc1 2f\n\t"
               "s_memrealtime vcc\n\ts_waitcnt lgkmcnt(0)\n\ts_mov_b32 %1, vcc_lo\n\ts_branch 1b\n2:"
               : "=&s"(a), "+s"(now) : "s"(lim), "n"(NN::value), "n"(CC) : "vcc", "scc", "memory");
}

__global__ void __launch_bounds__(512, 2)
fused_decode(const u16* x, const u16* kc, const u16* vc, const u16* wq, const u16* wo, const u16* wg, const u16* wu, const u16* wd,
             u16* out, unsigned char* ws) {
  __shared__ __attribute__((aligned(16))) unsigned char lds[L_END];
  u64 pc; u32 clen;
  asm volatile("s_getpc_b64 %0\n.Lcode_lo:\n\ts_mov_b32 %1, .Lcode_hi-.Lcode_lo" : "=s"(pc), "=s"(clen));
  const u32 cpf = raw_load_b32(v4u{(u32)pc, (u32)(pc >> 32) & 0xffffu, clen, 0x00020000u}, threadIdx.x * 2048 + blockIdx.x * 8, 0, 0);
  PIN();
  const int tid = threadIdx.x, l = tid & 63, w = __builtin_amdgcn_readfirstlane(tid >> 6);
  const int bid = blockIdx.x, xg = bid & 7, idx = bid >> 3;
  const int r = l >> 3, c = l & 7, g = l >> 4, col = l & 15;
  const bool pf = idx >= 26;
  const int ks = idx & 1, pu = idx >> 1, pcol = pu * 8 + xg - (pf ? 104 : 0);
  const u32 pstr = pf ? F_ * 2 : HD * 2;
  const u32 op = (u32)(r * 10 + 320 * ks + 80 * w) * pstr + (u32)(pcol * 128 + c * 16) + (pu == 12 ? OOB : 0u);
  const u32 oxl = (u32)(tid & 0x13f) * 40u;
  u32 vzero;
  asm("v_mov_b32 %0, 0" : "=v"(vzero));
  asm volatile("" :: "s"(x), "s"(kc), "s"(vc), "s"(wq), "s"(wo), "s"(wg), "s"(wu), "s"(wd), "s"(out), "s"(ws), "v"(op), "v"(oxl));
  const u32 tk = clk();
  const rsrc_t rX = mk(x, 2 * D_ * 2);
  const rsrc_t rW = mk(ws, WS_END);
  const v4u xa = __builtin_amdgcn_raw_buffer_load_b128(rX, oxl, 0, 0);
  const v4u xb = __builtin_amdgcn_raw_buffer_load_b128(rX, oxl + 16, 0, 0);
  const v2u xc = __builtin_amdgcn_raw_buffer_load_b64(rX, oxl + 32, 0, 0);
  const u32 e = __builtin_amdgcn_raw_buffer_load_b32(rW, vzero, WS_E, AUX_SC1);
  PIN();
  int h, b, sp, G, lc;
  bool tile;
  auto setup = [&]() {
    int i2 = idx;
    asm volatile("" : "+s"(i2));
    h = ((i2 >> 2) & 3) * 8 + xg; b = i2 >> 4; sp = i2 & 3;
    tile = i2 < 30; G = xg * 3 + i2 / 10; lc = i2 % 10;
  };
  auto tag_of = [&](u32 ep) { return ep ^ (((u32)dispatch_id() * 0x9E3779B1u) | 0x11111111u); };
  auto wait_flag = [&](int off, auto SL) {
    _Pragma("unroll 1") for (int spin = 0; spin < SPIN_CAP * 64 / (SL + 2); ++spin) {
      asm volatile("" ::: "memory");
      if (__builtin_amdgcn_readfirstlane(*(const u32*)(lds + off)) != SENT) break;
      __builtin_amdgcn_s_sleep(SL.value);
    }
    asm volatile("" ::: "memory");
  };

  v4u WO[4];
  auto issWO = [&](bool on) {
    const u32 o = (u32)((256 * G + 32 * w + 4 * r) * (D_ * 2) + lc * 128 + c * 16) | (tile && on ? 0u : OOB);
    sfor<0, 4>([&](auto N) { WO[N] = __builtin_amdgcn_raw_buffer_load_b128(mk(wo, HD * D_ * 2), o, N * (D_ * 2), AUX_NT); });
    PIN();
  };
  auto line_end = [&]() { asm volatile("" ::: "memory"); return __builtin_amdgcn_readfirstlane(*(const u32*)(lds + L_TB + 63 * 4)) + (TOFF + 63 * TAU / 16); };
  u32 m0, m1, oo, lrs;
  auto hop1_setup = [&]() {
    const u32 hm = (l & 1) ? 0xffff0000u : 0x0000ffffu;
    m0 = (l & 2) ? 0u : hm; m1 = (l & 2) ? hm : 0u;
    const int R0 = 256 * G + 32 * w, hh = R0 / DH;
    oo = (u32)((hh * 8 + (col >> 3) * 4 + (l & 3)) * REC + R_O + (R0 - hh * DH + 4 * (2 * g + ((col >> 2) & 1))) * 4);
    lrs = (g & 1) == (col >> 3) ? (u32)(L_RS + w * 1024 + (g & 1) * 512 + (g >> 1) * 256 + c * 32) : (u32)(L_SP + l * 32);
    asm volatile("" : "+v"(m0), "+v"(m1), "+v"(oo), "+v"(lrs));
  };
  auto hop1 = [&](u32 T, u32 tgate) {
    while ((int)(clk() - tgate) < 0) {}
    v4u o4;
    SPIN {
      asm volatile("" ::: "memory");
      o4 = __builtin_amdgcn_raw_buffer_load_b128(rW, oo, 0, AUX_SC1);
      if (__builtin_amdgcn_ballot_w64(tag4(o4) != T) == 0ull) break;
    }
    float v[4];
    sfor<0, 4>([&](auto E) { v[E] = dec(o4[E.value]); });
    __builtin_amdgcn_sched_barrier(0);
    sfor<0, 4>([&](auto E) { v[E] += dpp<XOR1>(v[E]); });
    __builtin_amdgcn_sched_barrier(0);
    sfor<0, 4>([&](auto E) { v[E] += dpp<XOR2>(v[E]); });
    __builtin_amdgcn_sched_barrier(0);
    v2u A[4];
    sfor<0, 4>([&](auto E) { const u32 d = pk(v[E], v[E]); A[E] = v2u{d & m0, d & m1}; });
    __builtin_amdgcn_sched_barrier(0);
    v4f lo = {0, 0, 0, 0}, hi = {0, 0, 0, 0};
    sfor<0, 4>([&](auto E) { lo = mfma16(A[E], v2u{WO[E][0], WO[E][1]}, lo); hi = mfma16(A[E], v2u{WO[E][2], WO[E][3]}, hi); });
    *(v4f*)(lds + lrs) = lo; *(v4f*)(lds + lrs + 16) = hi;
    wg_barrier();
  };

  if (w >= 4) {
    const int sw = w - 4;
    wg_barrier();
    setup();
    const int bh = b * H_ + h;
    const u32 okv = (u32)((sp * 32 + sw * 8 + r) * 384 + c * 16);
    auto kvstep = [&](int i) { return (u32)(((i + bh * ROT) & 31) * 49152); };
    const rsrc_t rK = mk((const unsigned char*)kc + (u32)(bh * (SK * DH * 2)), SK * DH * 2);
    const rsrc_t rV = mk((const unsigned char*)vc + (u32)(bh * (SK * DH * 2)), SK * DH * 2);
    v4u KV[64][3];
    u32 zt[3];
    {
      const u32 o3 = (u32)(l * 524288 - (l == 3 ? 4 : 0)) | (l < 4 ? 0u : OOB);
      zt[0] = __builtin_amdgcn_raw_buffer_load_b32(rK, o3 | (sw == 0 ? 0u : OOB), 0, AUX_NT);
      zt[1] = __builtin_amdgcn_raw_buffer_load_b32(rV, o3 | (sw == 1 ? 0u : OOB), 0, AUX_NT);
      zt[2] = __builtin_amdgcn_raw_buffer_load_b32(mk(wo, HD * D_ * 2), (u32)((256 * G + 255 * l) * (D_ * 2)) | ((l < 2 && sw == 2) ? 0u : OOB), 0, AUX_NT);
      PIN();
    }
    const int i0 = ((l + 1 - b) & ~1) + b - 1, ph = (bh >> 1) & 15;
    const int tp = TOFF + ((i0 * 16 + (i0 < 63 ? ph : ph * 3 / 4 - 5)) * TAU >> 8);
    const int tl = tp - N0 * CC, tm = tp + (C0 + 3) * (TAU / 48), otb = (i0 > 0 ? i0 : 0) * 4;
    u32 tb = *(const u32*)(lds + L_TB + l * 4);
    u32 tnow = clk();
    const u32 orep = l == 0 ? (u32)((b * 8 + xg) * PSTR) : OOB;
    const int rep = sw == 0 && idx == 5 ? 0 : sw == 0 && idx == 22 ? 1 : -1;
    auto iss = [&](auto I) {
      pace(__builtin_amdgcn_readlane(tb + tl, I), tnow);
      constexpr int J = (I + 1) & ~1;
      if constexpr (J >= CK0 && (J - CK0) % CKS == 0 && (J - CK0) / CKS < NCK) {
        if (rep == (I & 1)) {
          const u32 now = clk(), v = now + vm_out() * (TAU / 48) - __builtin_amdgcn_readlane(tm, I);
          if ((int)(now - __builtin_amdgcn_readlane(tb, I)) < TOFF + (J + CKD - RLEAD) * TAU / 16 - LATE)
            __builtin_amdgcn_raw_buffer_store_b64(v2u{v, tag_of(e)}, rW, orep, WS_P + (J - CK0) / CKS * 16 * PSTR, AUX_SC1);
        }
      }
      sfor<0, 3>([&](auto P) { KV[I][P] = __builtin_amdgcn_raw_buffer_load_b128(I < 32 ? rK : rV, okv + P * 128, kvstep(I), AUX_NT); });
      if constexpr (I >= CK0 + CKD - 2) tb = *(const u32*)(lds + L_TB + otb);
      PIN();
    };
    sfor<0, NPRE>(iss);
    asm volatile("" :: "v"(zt[0]), "v"(zt[1]), "v"(zt[2]), "v"(cpf), "v"(e));
    wait_flag(L_FQ, IC(1));
    v4u q[3];
    sfor<0, 3>([&](auto P) { q[P] = *(const v4u*)(lds + L_Q + P * 128 + c * 16); });
    PIN();
    float sc[32], mw;
    sfor<0, 32>([&](auto I) {
      float d0 = 0.f, d1 = 0.f, d2 = 0.f;
      sfor<0, 4>([&](auto E) { d0 = dot2(KV[I][0][E.value], q[0][E.value], d0); d1 = dot2(KV[I][1][E.value], q[1][E.value], d1); d2 = dot2(KV[I][2][E.value], q[2][E.value], d2); });
      sc[I] = rbf(rbf(sum8(d0 + d1 + d2)) * 0.07216878364870322f);
      mw = I == 0 ? sc[I] : fmaxf(mw, sc[I]);
      asm volatile("" : "+v"(sc[I]), "+v"(mw));
      PIN();
      sfor<issued_before(I), issued_before(I + 1)>(iss);
    });
    mw = fmaxf(mw, dpp<ROR8>(mw)); mw = max32(max16(mw));
    v4u msk;
    sfor<0, 4>([&](auto I) { msk[I.value] = (c >> 1) == I ? ((c & 1) ? 0xffff0000u : 0x0000ffffu) : 0u; });
    float lw = 0.f;
    u32 pp[32], Tq, lro, tgate;
    v4f acc[3] = {{0, 0, 0, 0}, {0, 0, 0, 0}, {0, 0, 0, 0}};
    sfor<32, 64>([&](auto I) {
      if constexpr (I < 36) {
        sfor<(I - 32) * 8, (I - 31) * 8>([&](auto N) { const float p = ex2((sc[N] - mw) * LOG2E); lw += p; pp[N] = pk(p, p); asm volatile("" : "+v"(pp[N])); });
        asm volatile("" : "+v"(lw));
      }
      if constexpr (I == 35) {
        lw += dpp<ROR8>(lw); lw = sum32(sum16(lw));
        *(v2u*)(lds + L_ML + sw * 8) = v2u{nosent(mw), nosent(lw)};
      }
      const v4u a = msk & pp[I - 32];
      sfor<0, 3>([&](auto P) { acc[P] = mfma32(a, KV[I][P], acc[P]); });
      if constexpr (I < 63) asm volatile("" : "+v"(acc[0]), "+v"(acc[1]), "+v"(acc[2]));
      PIN();
      sfor<issued_before(I), issued_before(I + 1)>(iss);
      if constexpr (I == GSTEP) { *(u32*)(lds + L_FP) = 1u; PIN(); }
      if constexpr (I == 50) {
        hop1_setup();
        Tq = tag_of(e);
        lro = (g >> 1) == (col >> 3) ? (u32)(L_RO + ((2 * sw + (g >> 1)) * 192 + c * 8 + 4 * (g & 1)) * 4) : (u32)(L_SP + l * 16);
        asm volatile("" : "+v"(Tq), "+v"(lro));
        PIN();
      }
      if constexpr (I == 60) {
        pace(line_end() + WOT - 15 * CC, tnow, IC(15));
        tgate = line_end() + TG;
        issWO(true);
      }
    });
    sfor<0, 3>([&](auto P) { *(v4f*)(lds + lro + P * 256) = acc[P]; });
    wg_barrier();
    if (tile) { __builtin_amdgcn_s_sleep(S1); hop1(Tq, tgate); }
    asm volatile(".Lcode_hi:");
    return;
  }

  v4u P0[10], P1[10], F2[8];
  {
    const rsrc_t rP = mk(pf ? wg : wq, D_ * pstr);
    sfor<0, 10>([&](auto I) { P0[I] = __builtin_amdgcn_raw_buffer_load_b128(rP, op, I * pstr, AUX_NT); __builtin_amdgcn_sched_barrier(0); });
    PIN();
  }
  setup();
  if (tid < 3) *(v4u*)(lds + L_ML + l * 16) = v4u{SENT, SENT, SENT, SENT};
  if (w == 1) {
    asm volatile("" :: "v"(e));
    PIN();
    const u32 d = clk() - tk;
    *(u32*)(lds + L_TB + l * 4) = tk + (d < TCAP ? d : TCAP);
  }
  wg_barrier();
  issWO(!pf);
  if (pf) sfor<0, 10>([&](auto I) { P1[I] = __builtin_amdgcn_raw_buffer_load_b128(mk(wu, D_ * F_ * 2), op, I * (F_ * 2), AUX_NT); });
  PIN();
  const u32 T = tag_of(e);
  {
    const u32 xd[10] = {xa[0], xa[1], xa[2], xa[3], xb[0], xb[1], xb[2], xb[3], xc[0], xc[1]};
    float s = 0.f;
    sfor<0, 10>([&](auto I) { s += lo16(xd[I]) + hi16(xd[I]); });
    const float mean = half_sum(s) * (1.f / D_);
    float v = 0.f;
    sfor<0, 10>([&](auto I) { const float d0 = lo16(xd[I]) - mean, d1 = hi16(xd[I]) - mean; v += d0 * d0 + d1 * d1; });
    const float var = half_sum(v) * (1.f / D_);
    const float mb = rbf(mean), inv = __builtin_amdgcn_rcpf(rbf(__builtin_sqrtf(rbf(rbf(var) + 1e-5f))));
    sfor<0, 5>([&](auto I) {
      const u32 d0 = pk(lo16(xd[2 * I]) - mb, hi16(xd[2 * I]) - mb), d1 = pk(lo16(xd[2 * I + 1]) - mb, hi16(xd[2 * I + 1]) - mb);
      *(v2u*)(lds + L_XN + w * 2560 + l * 40 + I * 8) = v2u{pk(lo16(d0) * inv, hi16(d0) * inv), pk(lo16(d1) * inv, hi16(d1) * inv)};
    });
    PIN();
  }
  sfor<0, 8>([&](auto I) { F2[I] = __builtin_amdgcn_raw_buffer_load_b128(mk(wd, F_ * D_ * 2), (u32)((64 * G + 8 * r) * (D_ * 2) + lc * 128 + c * 16) | ((w == 2 && tile) ? 0u : OOB), I * (D_ * 2), AUX_NT); });
  PIN();
  const u32 t0 = __builtin_amdgcn_readfirstlane(*(const u32*)(lds + L_TB));
  auto wait_clock = [&](int ticks) { while ((int)(clk() - t0) < ticks) __builtin_amdgcn_s_sleep(2); };
  const int ab = col >> 3, ar = 2 * g + ((col >> 2) & 1);
  hop1_setup();
  auto gemv = [&](u32 d, v4u W, v4f& lo, v4f& hi) { const v2u A = {d & m0, d & m1}; lo = mfma16(A, v2u{W[0], W[1]}, lo); hi = mfma16(A, v2u{W[2], W[3]}, hi); };
  const bool dv = (g & 1) == (col >> 3);
  const u32 ld0 = (u32)((g & 1) * 512 + (g >> 1) * 256 + c * 32);
  auto put = [&](int slot, v4f lo, v4f hi) { if (dv) { *(v4f*)(lds + L_RS + slot * 1024 + ld0) = lo; *(v4f*)(lds + L_RS + slot * 1024 + ld0 + 16) = hi; } };
  auto sum4 = [&](int slot) {
    v4f t[4];
    sfor<0, 4>([&](auto I) { t[I] = *(const v4f*)(lds + L_RS + (slot + I) * 1024 + l * 16); });
    v4f s = (t[0] + t[1]) + (t[2] + t[3]);
    sfor<0, 4>([&](auto E) { s[E.value] = sum32(s[E.value]); });
    return s;
  };
  {
    u32 xv[5];
    sfor<0, 5>([&](auto I) { xv[I] = *(const u32*)(lds + L_XN + w * 2560 + ab * 1280 + (320 * ks + 80 * w + 10 * ar) * 2 + I * 4); });
    v4f lo = {0, 0, 0, 0}, hi = {0, 0, 0, 0};
    sfor<0, 10>([&](auto I) { gemv(dup<I>(xv[I / 2]), P0[I], lo, hi); });
    put(w, lo, hi);
    if (pf) {
      lo = v4f{0, 0, 0, 0}; hi = v4f{0, 0, 0, 0};
      sfor<0, 10>([&](auto I) { gemv(dup<I>(xv[I / 2]), P1[I], lo, hi); });
      put(4 + w, lo, hi);
    }
    asm volatile("" ::: "memory");
    u32 n = 0;
    if (l == 0) n = __hip_atomic_fetch_add((u32*)(lds + L_CNT), 1u, __ATOMIC_RELAXED, __HIP_MEMORY_SCOPE_WORKGROUP);
    if (__builtin_amdgcn_readfirstlane(n) == SENT + 3u) {
      asm volatile("" ::: "memory");
      const u32 o = pf ? (u32)(WS_F + (pcol * 2 + ks) * 1024 + l * 16)
                       : (u32)(WS_Q + ks * 49152 + (l >> 4) * 24576 + pcol * 256 + (l & 15) * 16);
      const u32 om = o | ((l < 32 && pu != 12) ? 0u : OOB);
      __builtin_amdgcn_raw_buffer_store_b128(enc4n(sum4(0), T, l & 1), rW, om, 0, AUX_SC1);
      if (pf) __builtin_amdgcn_raw_buffer_store_b128(enc4n(sum4(4), T, l & 1), rW, om, 512, AUX_SC1);
    }
    PIN();
  }
  if (pf) issWO(true);
  const int me = h * 8 + b * 4 + sp;
  const bool fin = w == 0 && bid < 40;
  const int fq = (bid & 3) * 8 + c;
  const u32 ofin = (u32)(((bid >> 2) * 24 + r) * 512 + fq * 16);
  const u32 oidx = (u32)(((fq >> 4) * D_ + (bid >> 2) * 64 + (fq & 15) * 4) * 2);
  u32 oout = (fin && l < 8) ? oidx : OOB;
  v4f fw = {0, 0, 0, 0};
  float gate = 0.f, xv = 0.f;

  if (w == 3) {
    wait_clock(QT);
    const v4f s = gather2<49152, 48>(rW, (u32)(WS_Q + b * 24576 + h * 768 + l * 16), l, T);
    if (l < 48) *(v2u*)(lds + L_Q + l * 8) = v2u{pk(s[0], s[1]), pk(s[2], s[3])};
    asm volatile("" ::: "memory");
    if (l == 0) *(u32*)(lds + L_FQ) = 1u;
  } else if (w == 2) {
    if (tile) {
      wait_clock(FT);
      const v4f s = gather2<1024, 64>(rW, (u32)(WS_F + G * 2048 + l * 16), l, T);
      float hv[4];
      sfor<0, 4>([&](auto E) {
        const auto t = __builtin_amdgcn_permlane32_swap(asu(s[E.value]), asu(s[E.value]), false, false);
        const float gg = rbf(asf(t[0])), uu = rbf(asf(t[1]));
        const float z = 0.7978845608028654f * (gg + 0.044715f * gg * gg * gg);
        hv[E] = rbf(gg / (1.f + ex2(-2.f * LOG2E * z))) * uu;
      });
      if (l < 32) *(v2u*)(lds + L_H + l * 8) = v2u{pk(hv[0], hv[1]), pk(hv[2], hv[3])};
      const v4u hh = *(const v4u*)(lds + L_H + ab * 128 + ar * 16);
      v4f lo = {0, 0, 0, 0}, hi = {0, 0, 0, 0};
      sfor<0, 8>([&](auto I) { gemv(dup<I>(hh[I / 2]), F2[I], lo, hi); });
      put(8, lo, hi);
      v4f y = *(const v4f*)(lds + L_RS + 8 * 1024 + l * 16);
      sfor<0, 4>([&](auto E) { y[E.value] = sum32(y[E.value]); });
      __builtin_amdgcn_raw_buffer_store_b128(enc4(y, T), rW, l < 32 ? (u32)(WS_Y + (lc * 24 + G) * 512 + l * 16) : OOB, 0, AUX_SC1);
    }
  } else if (w == 1) {
    int base = 0, m1_ = 0, m2_ = 0;
    bool tilted = false;
    _Pragma("unroll 1") for (int k = 0; k < NCK; ++k) {
      const int S = CK0 + k * CKS, E = S + CKD, end = S < 32 ? 32 : 64, bS = __builtin_amdgcn_readlane(base, S);
      wait_clock(__builtin_amdgcn_readlane(base, E - RLEAD) + TOFF + (E - RLEAD) * TAU / 16);
      asm volatile("" ::: "memory");
      const v2u z = __builtin_amdgcn_raw_buffer_load_b64(rW, (u32)(WS_P + (k * 16 + col) * PSTR), 0, AUX_SC1);
      int v = (int)(z[0] - t0);
      if (z[1] != T || (u32)(v + 1000) > 2000u) v = bS + MISS;
      v += __builtin_amdgcn_mov_dpp(v, XOR1, 0xf, 0xf, true); v += __builtin_amdgcn_mov_dpp(v, XOR2, 0xf, 0xf, true);
      v += __builtin_amdgcn_mov_dpp(v, HMIR, 0xf, 0xf, true); v += __builtin_amdgcn_mov_dpp(v, ROR8, 0xf, 0xf, true);
      const int m = __builtin_amdgcn_readfirstlane(v) >> 4;
      if (S == 32) tilted = false;
      if (k == 0) base = l >= E ? m - (l > XS ? (l - XS) * XV / 16 : 0) : 0;
      else {
        const int bl = m - __builtin_amdgcn_readlane(base, E + 3 < 63 ? E + 3 : 63);
        int d = 0;
        if (k == 1) d = bl - KEEP1;
        else if (!tilted && (S < 32 || S >= 40) && m - m2_ >= (S == 12 ? GK12 : S == 16 ? GK16 : S < 32 ? GK : GV) && m - bS >= LV) {
          d = bl + GROW * (CKD < end - S ? CKD : end - S) - REST;
          const int n2 = (l < end ? l : end) - E;
          base += n2 > 0 ? n2 * (2 * SIG + (S < 32 ? 0 : XV)) / 16 : 0;
          tilted = true;
        } else if (bl > LMAX - (S > 32 ? (S - 32) * USL / 16 : 0)) d = bl - KEEP;
        const int rp = RAMP * (l - (E - 1));
        if (d > 0) base += rp < 0 ? 0 : rp < d ? rp : d;
      }
      m2_ = m1_; m1_ = m;
      *(u32*)(lds + L_TB + l * 4) = t0 + base;
    }
  } else {
    wait_flag(L_FQ, IC(16));
    xv = lo16(__builtin_amdgcn_raw_buffer_load_b16(rX, fin ? oidx + g * 2 : OOB, 0, 0));
    {
      const u32 z = __builtin_amdgcn_raw_buffer_load_b32(rW, l < 6 ? (u32)(me * REC + R_O + l * 128) : (l < 10 && tile) ? (u32)(WS_S + (lc * 24 + G) * 512 + (l - 6) * 128) : OOB, 0, AUX_SC1);
      asm volatile("" :: "v"(z));
    }
    __builtin_amdgcn_raw_buffer_store_b64(v2u{0u, 0u}, mk(out, 2 * D_ * 2), oout, 0, AUX_SC1);
    v2u mlw;
    _Pragma("unroll 1") for (int spin = 0; spin < SPIN_CAP * 4; ++spin) {
      asm volatile("" ::: "memory");
      mlw = *(const v2u*)(lds + L_ML + (l & 3) * 8);
      if (__all(mlw[0] != SENT && mlw[1] != SENT)) break;
      __builtin_amdgcn_s_sleep(16);
    }
    float Mg = asf(mlw[0]); Mg = fmaxf(Mg, dpp<XOR1>(Mg)); Mg = fmaxf(Mg, dpp<XOR2>(Mg));
    float Lg = asf(mlw[1]) * ex2((asf(mlw[0]) - Mg) * LOG2E); Lg += dpp<XOR1>(Lg); Lg += dpp<XOR2>(Lg);
    __builtin_amdgcn_raw_buffer_store_b128(enc4(v4f{Mg, Lg, 0.f, 0.f}, T), rW, l < 8 ? (u32)(me * REC + R_ML + l * 16) : OOB, 0, AUX_SC1);
    if (fin) {
      const float y = rbf(final_sum<16>(rW, ofin, WS_Y, T));
      gate = rbf(y / (1.f + ex2(-LOG2E * y)));
    }
    wait_flag(L_FP, IC(16));
    v4u ml4;
    SPIN {
      asm volatile("" ::: "memory");
      ml4 = __builtin_amdgcn_raw_buffer_load_b128(rW, (u32)((h * 8 + b * 4 + (l & 3)) * REC + R_ML + ((l >> 2) & 7) * 16), 0, AUX_SC1);
      if (__all(tag4(ml4) == T)) break;
      __builtin_amdgcn_s_sleep(16);
    }
    float M = dec(ml4[0]); M = fmaxf(M, dpp<XOR1>(M)); M = fmaxf(M, dpp<XOR2>(M));
    float L = dec(ml4[1]) * ex2((dec(ml4[0]) - M) * LOG2E); L += dpp<XOR1>(L); L += dpp<XOR2>(L);
    const float f = ex2((asf(mlw[0]) - M) * LOG2E) / L;
    fw = v4f{dpp<0x00>(f), dpp<0x55>(f), dpp<0xAA>(f), dpp<0xFF>(f)};
  }

  asm volatile("" :: "v"(cpf));
  __builtin_amdgcn_s_waitcnt(0x0F70);
  u32 ost = l < 48 ? (u32)(me * REC + R_O + l * 16) : OOB, oss = l < 32 ? (u32)(WS_S + (lc * 24 + G) * 512 + l * 16) : OOB;
  u32 oep = (bid == 0 && l == 0) ? (u32)WS_E : OOB;
  asm volatile("" : "+v"(ost), "+v"(oss), "+v"(oout), "+v"(oep), "+v"(xv), "+v"(fw), "+v"(gate));
  wg_barrier();
  if (w == 0) {
    v4f t[8];
    sfor<0, 8>([&](auto I) { t[I] = *(const v4f*)(lds + L_RO + I * 768 + l * 16); });
    __builtin_amdgcn_sched_barrier(0);
    const v4f s = ((t[0] + t[1]) * fw[0] + (t[2] + t[3]) * fw[1]) + ((t[4] + t[5]) * fw[2] + (t[6] + t[7]) * fw[3]);
    __builtin_amdgcn_raw_buffer_store_b128(enc4(s, T), rW, ost, 0, AUX_SC1);
  } else {
    __builtin_amdgcn_s_sleep(S1 - S0 - 1);
  }
  if (tile) {
    __builtin_amdgcn_s_sleep(S0);
    hop1(T, line_end() + TG);
    if (w == 0) {
      v4f t[8];
      sfor<0, 8>([&](auto I) { t[I] = *(const v4f*)(lds + L_RS + I * 1024 + l * 16); });
      __builtin_amdgcn_sched_barrier(0);
      v4f s = ((t[0] + t[1]) + (t[2] + t[3])) + ((t[4] + t[5]) + (t[6] + t[7]));
      sfor<0, 4>([&](auto E) { s[E.value] = sum32(s[E.value]); });
      __builtin_amdgcn_raw_buffer_store_b128(enc4(s, T), rW, oss, 0, AUX_SC1);
      if (fin) {
        __builtin_amdgcn_s_sleep(S2);
        const float o = xv + rbf(rbf(final_sum<0>(rW, ofin, WS_S, T)) * gate);
        const auto p2 = __builtin_amdgcn_permlane16_swap(asu(o), asu(o), false, false);
        const u32 d2 = pk(asf(p2[0]), asf(p2[1]));
        const auto p4 = __builtin_amdgcn_permlane32_swap(d2, d2, false, false);
        __builtin_amdgcn_raw_buffer_store_b64(v2u{p4[0], p4[1]}, mk(out, 2 * D_ * 2), oout, 0, AUX_SC1);
        __builtin_amdgcn_raw_buffer_store_b32(T, rW, oep, 0, AUX_SC1);
      }
    }
  }
}

extern "C" void kernel_launch(void* const* d_in, const int* in_sizes, int n_in, void* d_out, int out_size, void* d_ws, size_t ws_size,
                              hipStream_t stream) {
  fused_decode<<<256, 512, 0, stream>>>((const u16*)d_in[0], (const u16*)d_in[1], (const u16*)d_in[2], (const u16*)d_in[3], (const u16*)d_in[4],
                                        (const u16*)d_in[6], (const u16*)d_in[7], (const u16*)d_in[8], (u16*)d_out, (unsigned char*)d_ws);
}
```

```cpp
#include <hip/hip_runtime.h>
#include <stdint.h>
#include <type_traits>

typedef unsigned u32;
typedef unsigned long long u64;
typedef unsigned short u16;
typedef u32 v4u __attribute__((ext_vector_type(4)));
typedef u32 v2u __attribute__((ext_vector_type(2)));
typedef float v4f __attribute__((ext_vector_type(4)));
typedef short s4 __attribute__((ext_vector_type(4)));
typedef __bf16 bf8 __attribute__((ext_vector_type(8)));
typedef __bf16 bf2 __attribute__((ext_vector_type(2)));
typedef __amdgpu_buffer_rsrc_t rsrc_t;

constexpr int D_ = 640, H_ = 32, SK = 4096, DH = 192, F_ = 1536, HD = H_ * DH;
constexpr int AUX_NT = 2, AUX_SC1 = 16;
constexpr u32 OOB = 0x80000000u;
constexpr u32 SENT = 0xFFFFFFFFu;
constexpr int SPIN_CAP = 1 << 18;

constexpr int WS_E = 0;
constexpr int REC = 896, R_O = 256, R_ML = R_O + 768;
constexpr int WS_Q = R_O + 256 * REC;
constexpr int WS_F = WS_Q + 64 * 1536;
constexpr int WS_Y = WS_F + 48 * 1024;
constexpr int WS_S = WS_Y + 240 * 512;
constexpr int WS_P = WS_S + 240 * 512, PSTR = 32;
constexpr int WS_END = WS_P + 16 * 16 * PSTR;

constexpr int NPRE = 14, WMIN = 6;
constexpr int QT = 410, FT = 1050;
constexpr int GSTEP = 44;
constexpr int TAU = 665, XV = 17, XS = 36;
constexpr int TOFF = 190;
constexpr int N0 = 6, CC = 40;
constexpr int ROT = 7;
constexpr int WOT = 55;
constexpr int S0 = 5, S1 = 10, TG = 135;
constexpr int S2 = 8;
constexpr int CK0 = 4, CKS = 4, NCK = 14;
constexpr int CKD = 7, RLEAD = 3;
constexpr int C0 = 2, LATE = 90, MISS = 130;
constexpr int KEEP1 = 28, LMAX = 46, USL = 8, KEEP = 18;
constexpr int GK12 = 68, GK16 = 40, GK = 30, GV = 18, LV = 26, GROW = 3, REST = 5, SIG = 23, RAMP = 16;

#define IC(n) std::integral_constant<int, (n)>{}

constexpr int L_XN = 0;
constexpr int L_Q = L_XN + 4 * 2560;
constexpr int L_H = L_Q + 384;
constexpr int L_ML = L_H + 256;
constexpr int L_FQ = L_ML + 32, L_FP = L_ML + 36, L_CNT = L_ML + 40;
constexpr int L_TB = L_ML + 64;
constexpr int L_RO = L_TB + 256;
constexpr int L_RS = L_RO + 8 * 768;
constexpr int L_SP = L_RS + 9 * 1024;
constexpr int L_END = L_SP + 2048;

template <int I, int N, class F> __device__ __forceinline__ void sfor(F&& f) {
  if constexpr (I < N) { f(std::integral_constant<int, I>{}); sfor<I + 1, N>(f); }
}
#define PIN() do { asm volatile("" ::: "memory"); __builtin_amdgcn_sched_barrier(0); } while (0)
#define SPIN _Pragma("unroll 1") for (int spin = 0; spin < SPIN_CAP; ++spin)
__device__ __forceinline__ void wg_barrier() {
  __builtin_amdgcn_sched_barrier(0);
  asm volatile("s_waitcnt lgkmcnt(0)\n\ts_barrier" ::: "memory");
  __builtin_amdgcn_sched_barrier(0);
}
__device__ __forceinline__ rsrc_t mk(const void* p, u32 bytes) { return __builtin_amdgcn_make_buffer_rsrc((void*)p, 0, bytes, 0x00020000); }
__device__ __forceinline__ float asf(u32 x) { return __builtin_bit_cast(float, x); }
__device__ __forceinline__ u32 asu(float x) { return __builtin_bit_cast(u32, x); }
__device__ __forceinline__ u32 pk(float lo, float hi) { bf2 v = {(__bf16)lo, (__bf16)hi}; return __builtin_bit_cast(u32, v); }
__device__ __forceinline__ float rbf(float x) { return (float)(__bf16)x; }
__device__ __forceinline__ float lo16(u32 x) { return asf(x << 16); }
__device__ __forceinline__ float hi16(u32 x) { return asf(x & 0xffff0000u); }
template <int C> __device__ __forceinline__ float dpp(float x) { return asf(__builtin_amdgcn_mov_dpp(asu(x), C, 0xf, 0xf, true)); }
constexpr int XOR1 = 0xB1, XOR2 = 0x4E, HMIR = 0x141, ROR8 = 0x128;
__device__ __forceinline__ float sum16(float x) { auto s = __builtin_amdgcn_permlane16_swap(asu(x), asu(x), false, false); return asf(s[0]) + asf(s[1]); }
__device__ __forceinline__ float sum32(float x) { auto s = __builtin_amdgcn_permlane32_swap(asu(x), asu(x), false, false); return asf(s[0]) + asf(s[1]); }
__device__ __forceinline__ float max16(float x) { auto s = __builtin_amdgcn_permlane16_swap(asu(x), asu(x), false, false); return fmaxf(asf(s[0]), asf(s[1])); }
__device__ __forceinline__ float max32(float x) { auto s = __builtin_amdgcn_permlane32_swap(asu(x), asu(x), false, false); return fmaxf(asf(s[0]), asf(s[1])); }
__device__ __forceinline__ float sum8(float x) { x += dpp<XOR1>(x); x += dpp<XOR2>(x); x += dpp<HMIR>(x); return x; }
__device__ __forceinline__ float half_sum(float x) { x = sum8(x); x += dpp<ROR8>(x); return sum16(x); }
__device__ __forceinline__ float dot2(u32 a, u32 b, float c) { return __builtin_amdgcn_fdot2_f32_bf16(__builtin_bit_cast(bf2, a), __builtin_bit_cast(bf2, b), c, false); }
__device__ __forceinline__ v4f mfma32(v4u a, v4u b, v4f c) { return __builtin_amdgcn_mfma_f32_16x16x32_bf16(__builtin_bit_cast(bf8, a), __builtin_bit_cast(bf8, b), c, 0, 0, 0); }
__device__ __forceinline__ v4f mfma16(v2u a, v2u b, v4f c) { return __builtin_amdgcn_mfma_f32_16x16x16bf16_1k(__builtin_bit_cast(s4, a), __builtin_bit_cast(s4, b), c, 0, 0, 0); }
template <int N> __device__ __forceinline__ u32 dup(u32 x) { return __builtin_amdgcn_perm(x, x, (N & 1) ? 0x03020302u : 0x01000100u); }
__device__ __forceinline__ u32 nosent(float x) { u32 u = asu(x); return u < 0xFFFFFFFEu ? u : 0xFFFFFFFEu; }
__device__ __forceinline__ float ex2(float x) { return __builtin_amdgcn_exp2f(x); }
constexpr float LOG2E = 1.4426950408889634f;

__device__ __forceinline__ v4u enc4(v4f x, u32 T) {
  return v4u{__builtin_amdgcn_perm(asu(x[0]), T, 0x07060500u), __builtin_amdgcn_perm(asu(x[1]), T, 0x07060501u),
             __builtin_amdgcn_perm(asu(x[2]), T, 0x07060502u), __builtin_amdgcn_perm(asu(x[3]), T, 0x07060503u)};
}
__device__ __forceinline__ u32 tag4(v4u x) { return __builtin_amdgcn_perm(x[1], x[0], 0x0c0c0400u) | __builtin_amdgcn_perm(x[3], x[2], 0x04000c0cu); }
__device__ __forceinline__ u32 tagdiff(v4u x, u32 T) { return __builtin_amdgcn_sad_u8(tag4(x), T, 0u); }
__device__ __forceinline__ float dec(u32 x) { return asf(x & 0xffffff00u); }
__device__ __forceinline__ v4u nibbles(u32 T, int odd) { const u32 t = T >> (16 * odd); return v4u{t & 15u, (t >> 4) & 15u, (t >> 8) & 15u, (t >> 12) & 15u}; }
__device__ __forceinline__ v4u enc4n(v4f x, u32 T, int odd) { return (__builtin_bit_cast(v4u, x) & 0xfffffff0u) | nibbles(T, odd); }
template <int STRIDE, int LANES> __device__ __forceinline__ v4f gather2(rsrc_t rW, u32 off, int l, u32 T) {
  v4u a, b;
  const v4u nb = nibbles(T, l & 1);
  SPIN {
    asm volatile("" ::: "memory");
    a = __builtin_amdgcn_raw_buffer_load_b128(rW, l < LANES ? off : OOB, 0, AUX_SC1);
    b = __builtin_amdgcn_raw_buffer_load_b128(rW, l < LANES ? off : OOB, STRIDE, AUX_SC1);
    const v4u m = (a ^ nb) | (b ^ nb);
    if (__all(l >= LANES || ((m[0] | m[1] | m[2] | m[3]) & 15u) == 0u)) break;
    __builtin_amdgcn_s_sleep(16);
  }
  return __builtin_bit_cast(v4f, a & 0xfffffff0u) + __builtin_bit_cast(v4f, b & 0xfffffff0u);
}
template <int SLEEP> __device__ __forceinline__ float final_sum(rsrc_t rW, u32 off, int base, u32 T) {
  v4u t[3];
  SPIN {
    asm volatile("" ::: "memory");
    sfor<0, 3>([&](auto I) { t[I] = __builtin_amdgcn_raw_buffer_load_b128(rW, off, base + I * 8 * 512, AUX_SC1); });
    if (__builtin_amdgcn_ballot_w64(tagdiff(t[0], T) + tagdiff(t[1], T) + tagdiff(t[2], T) != 0u) == 0ull) break;
    if (SLEEP) __builtin_amdgcn_s_sleep(SLEEP);
  }
  const v4f s = __builtin_bit_cast(v4f, t[0] & 0xffffff00u) + __builtin_bit_cast(v4f, t[1] & 0xffffff00u) + __builtin_bit_cast(v4f, t[2] & 0xffffff00u);
  const auto a = __builtin_amdgcn_permlane32_swap(asu(s[0]), asu(s[2]), false, false), b = __builtin_amdgcn_permlane32_swap(asu(s[1]), asu(s[3]), false, false);
  const auto d = __builtin_amdgcn_permlane16_swap(asu(asf(a[0]) + asf(a[1])), asu(asf(b[0]) + asf(b[1])), false, false);
  const float v = asf(d[0]) + asf(d[1]);
  return v + dpp<ROR8>(v);
}
constexpr int win_of(int u) { return NPRE - u / 3 > WMIN ? NPRE - u / 3 : WMIN; }
constexpr int issued_before(int u) { return u + win_of(u) < 64 ? u + win_of(u) : 64; }
extern "C" __device__ u32 raw_load_b32(v4u, u32, u32, u32) __asm("llvm.amdgcn.raw.buffer.load.i32");
extern "C" __device__ u64 dispatch_id() __asm("llvm.amdgcn.dispatch.id");
__device__ __forceinline__ u32 clk() { return (u32)__builtin_amdgcn_s_memrealtime(); }
__device__ __forceinline__ u32 vm_out() { return __builtin_amdgcn_s_getreg(7 | (3 << 11)); }
template <class NN = std::integral_constant<int, N0>> __device__ __forceinline__ void pace(u32 lim, u32& now, NN = NN{}) {
  u32 a;
  asm volatile("1:\n\ts_getreg_b32 %0, hwreg(HW_REG_IB_STS, 0, 4)\n\ts_max_u32 %0, %0, %3\n\ts_mulk_i32 %0, %4\n\ts_add_u32 %0, %0, %2\n\t"
               "s_sub_u32 %0, %1, %0\n\ts_cmp_ge_i32 %0, 0\n\ts_cbranch_scc1 2f\n\t"
               "s_memrealtime vcc\n\ts_waitcnt lgkmcnt(0)\n\ts_mov_b32 %1, vcc_lo\n\ts_branch 1b\n2:"
               : "=&s"(a), "+s"(now) : "s"(lim), "n"(NN::value), "n"(CC) : "vcc", "scc", "memory");
}

__global__ void __launch_bounds__(512, 2)
fused_decode(const u16* x, const u16* kc, const u16* vc, const u16* wq, const u16* wo, const u16* wg, const u16* wu, const u16* wd,
             u16* out, unsigned char* ws) {
  __shared__ __attribute__((aligned(16))) unsigned char lds[L_END];
  u64 pc; u32 clen;
  asm volatile("s_getpc_b64 %0\n.Lcode_lo:\n\ts_mov_b32 %1, .Lcode_hi-.Lcode_lo" : "=s"(pc), "=s"(clen));
  const u32 cpf = raw_load_b32(v4u{(u32)pc, (u32)(pc >> 32) & 0xffffu, clen, 0x00020000u}, threadIdx.x * 2048 + blockIdx.x * 8, 0, 0);
  PIN();
  const int tid = threadIdx.x, l = tid & 63, w = __builtin_amdgcn_readfirstlane(tid >> 6);
  const int bid = blockIdx.x, xg = bid & 7, idx = bid >> 3;
  const int r = l >> 3, c = l & 7, g = l >> 4, col = l & 15;
  const bool pf = idx >= 26;
  const int ks = idx & 1, pu = idx >> 1, pcol = pu * 8 + xg - (pf ? 104 : 0);
  const u32 pstr = pf ? F_ * 2 : HD * 2;
  const u32 op = (u32)(r * 10 + 320 * ks + 80 * w) * pstr + (u32)(pcol * 128 + c * 16) + (pu == 12 ? OOB : 0u);
  const u32 oxl = (u32)(tid & 0x13f) * 40u;
  u32 vzero;
  asm("v_mov_b32 %0, 0" : "=v"(vzero));
  asm volatile("" :: "s"(x), "s"(kc), "s"(vc), "s"(wq), "s"(wo), "s"(wg), "s"(wu), "s"(wd), "s"(out), "s"(ws), "v"(op), "v"(oxl));
  const rsrc_t rX = mk(x, 2 * D_ * 2);
  const rsrc_t rW = mk(ws, WS_END);
  const v4u xa = __builtin_amdgcn_raw_buffer_load_b128(rX, oxl, 0, 0);
  const v4u xb = __builtin_amdgcn_raw_buffer_load_b128(rX, oxl + 16, 0, 0);
  const v2u xc = __builtin_amdgcn_raw_buffer_load_b64(rX, oxl + 32, 0, 0);
  const u32 e = __builtin_amdgcn_raw_buffer_load_b32(rW, vzero, WS_E, AUX_SC1);
  PIN();
  int h, b, sp, G, lc;
  bool tile;
  auto setup = [&]() {
    int i2 = idx;
    asm volatile("" : "+s"(i2));
    h = ((i2 >> 2) & 3) * 8 + xg; b = i2 >> 4; sp = i2 & 3;
    tile = i2 < 30; G = xg * 3 + i2 / 10; lc = i2 % 10;
  };
  auto tag_of = [&](u32 ep) { return ep ^ (((u32)dispatch_id() * 0x9E3779B1u) | 0x11111111u); };
  auto wait_flag = [&](int off, auto SL) {
    _Pragma("unroll 1") for (int spin = 0; spin < SPIN_CAP * 64 / (SL + 2); ++spin) {
      asm volatile("" ::: "memory");
      if (__builtin_amdgcn_readfirstlane(*(const u32*)(lds + off)) != SENT) break;
      __builtin_amdgcn_s_sleep(SL.value);
    }
    asm volatile("" ::: "memory");
  };

  v4u WO[4];
  auto issWO = [&](bool on) {
    const u32 o = (u32)((256 * G + 32 * w + 4 * r) * (D_ * 2) + lc * 128 + c * 16) | (tile && on ? 0u : OOB);
    sfor<0, 4>([&](auto N) { WO[N] = __builtin_amdgcn_raw_buffer_load_b128(mk(wo, HD * D_ * 2), o, N * (D_ * 2), AUX_NT); });
    PIN();
  };
  auto line_end = [&]() { asm volatile("" ::: "memory"); return __builtin_amdgcn_readfirstlane(*(const u32*)(lds + L_TB + 63 * 4)) + (TOFF + 63 * TAU / 16); };
  u32 m0, m1, oo, lrs;
  auto hop1_setup = [&]() {
    const u32 hm = (l & 1) ? 0xffff0000u : 0x0000ffffu;
    m0 = (l & 2) ? 0u : hm; m1 = (l & 2) ? hm : 0u;
    const int R0 = 256 * G + 32 * w, hh = R0 / DH;
    oo = (u32)((hh * 8 + (col >> 3) * 4 + (l & 3)) * REC + R_O + (R0 - hh * DH + 4 * (2 * g + ((col >> 2) & 1))) * 4);
    lrs = (g & 1) == (col >> 3) ? (u32)(L_RS + w * 1024 + (g & 1) * 512 + (g >> 1) * 256 + c * 32) : (u32)(L_SP + l * 32);
    asm volatile("" : "+v"(m0), "+v"(m1), "+v"(oo), "+v"(lrs));
  };
  auto hop1 = [&](u32 T, u32 tgate) {
    while ((int)(clk() - tgate) < 0) {}
    v4u o4;
    SPIN {
      asm volatile("" ::: "memory");
      o4 = __builtin_amdgcn_raw_buffer_load_b128(rW, oo, 0, AUX_SC1);
      if (__builtin_amdgcn_ballot_w64(tag4(o4) != T) == 0ull) break;
    }
    float v[4];
    sfor<0, 4>([&](auto E) { v[E] = dec(o4[E.value]); });
    __builtin_amdgcn_sched_barrier(0);
    sfor<0, 4>([&](auto E) { v[E] += dpp<XOR1>(v[E]); });
    __builtin_amdgcn_sched_barrier(0);
    sfor<0, 4>([&](auto E) { v[E] += dpp<XOR2>(v[E]); });
    __builtin_amdgcn_sched_barrier(0);
    v2u A[4];
    sfor<0, 4>([&](auto E) { const u32 d = pk(v[E], v[E]); A[E] = v2u{d & m0, d & m1}; });
    __builtin_amdgcn_sched_barrier(0);
    v4f lo = {0, 0, 0, 0}, hi = {0, 0, 0, 0};
    sfor<0, 4>([&](auto E) { lo = mfma16(A[E], v2u{WO[E][0], WO[E][1]}, lo); hi = mfma16(A[E], v2u{WO[E][2], WO[E][3]}, hi); });
    *(v4f*)(lds + lrs) = lo; *(v4f*)(lds + lrs + 16) = hi;
    wg_barrier();
  };

  if (w >= 4) {
    const int sw = w - 4;
    wg_barrier();
    setup();
    const int bh = b * H_ + h;
    const u32 okv = (u32)((sp * 32 + sw * 8 + r) * 384 + c * 16);
    auto kvstep = [&](int i) { return (u32)(((i + bh * ROT) & 31) * 49152); };
    const rsrc_t rK = mk((const unsigned char*)kc + (u32)(bh * (SK * DH * 2)), SK * DH * 2);
    const rsrc_t rV = mk((const unsigned char*)vc + (u32)(bh * (SK * DH * 2)), SK * DH * 2);
    v4u KV[64][3];
    u32 zt[3];
    {
      const u32 o3 = (u32)(l * 524288 - (l == 3 ? 4 : 0)) | (l < 4 ? 0u : OOB);
      zt[0] = __builtin_amdgcn_raw_buffer_load_b32(rK, o3 | (sw == 0 ? 0u : OOB), 0, AUX_NT);
      zt[1] = __builtin_amdgcn_raw_buffer_load_b32(rV, o3 | (sw == 1 ? 0u : OOB), 0, AUX_NT);
      zt[2] = __builtin_amdgcn_raw_buffer_load_b32(mk(wo, HD * D_ * 2), (u32)((256 * G + 255 * l) * (D_ * 2)) | ((l < 2 && sw == 2) ? 0u : OOB), 0, AUX_NT);
      PIN();
    }
    const int i0 = ((l + 1 - b) & ~1) + b - 1, ph = (bh >> 1) & 15;
    const int tp = TOFF + ((i0 * 16 + (i0 < 63 ? ph : ph * 3 / 4 - 5)) * TAU >> 8);
    const int tl = tp - N0 * CC, tm = tp + (C0 + 3) * (TAU / 48), otb = (i0 > 0 ? i0 : 0) * 4;
    u32 tb = *(const u32*)(lds + L_TB + l * 4);
    u32 tnow = clk();
    const u32 orep = l == 0 ? (u32)((b * 8 + xg) * PSTR) : OOB;
    const int rep = sw == 0 && idx == 5 ? 0 : sw == 0 && idx == 22 ? 1 : -1;
    auto iss = [&](auto I) {
      pace(__builtin_amdgcn_readlane(tb + tl, I), tnow);
      constexpr int J = (I + 1) & ~1;
      if constexpr (J >= CK0 && (J - CK0) % CKS == 0 && (J - CK0) / CKS < NCK) {
        if (rep == (I & 1)) {
          const u32 now = clk(), v = now + vm_out() * (TAU / 48) - __builtin_amdgcn_readlane(tm, I);
          if ((int)(now - __builtin_amdgcn_readlane(tb, I)) < TOFF + (J + CKD - RLEAD) * TAU / 16 - LATE)
            __builtin_amdgcn_raw_buffer_store_b64(v2u{v, tag_of(e)}, rW, orep, WS_P + (J - CK0) / CKS * 16 * PSTR, AUX_SC1);
        }
      }
      sfor<0, 3>([&](auto P) { KV[I][P] = __builtin_amdgcn_raw_buffer_load_b128(I < 32 ? rK : rV, okv + P * 128, kvstep(I), AUX_NT); });
      if constexpr (I >= CK0 + CKD - 2) tb = *(const u32*)(lds + L_TB + otb);
      PIN();
    };
    sfor<0, NPRE>(iss);
    asm volatile("" :: "v"(zt[0]), "v"(zt[1]), "v"(zt[2]), "v"(cpf), "v"(e));
    wait_flag(L_FQ, IC(1));
    v4u q[3];
    sfor<0, 3>([&](auto P) { q[P] = *(const v4u*)(lds + L_Q + P * 128 + c * 16); });
    PIN();
    float sc[32], mw;
    sfor<0, 32>([&](auto I) {
      float d0 = 0.f, d1 = 0.f, d2 = 0.f;
      sfor<0, 4>([&](auto E) { d0 = dot2(KV[I][0][E.value], q[0][E.value], d0); d1 = dot2(KV[I][1][E.value], q[1][E.value], d1); d2 = dot2(KV[I][2][E.value], q[2][E.value], d2); });
      sc[I] = rbf(rbf(sum8(d0 + d1 + d2)) * 0.07216878364870322f);
      mw = I == 0 ? sc[I] : fmaxf(mw, sc[I]);
      asm volatile("" : "+v"(sc[I]), "+v"(mw));
      PIN();
      sfor<issued_before(I), issued_before(I + 1)>(iss);
    });
    mw = fmaxf(mw, dpp<ROR8>(mw)); mw = max32(max16(mw));
    v4u msk;
    sfor<0, 4>([&](auto I) { msk[I.value] = (c >> 1) == I ? ((c & 1) ? 0xffff0000u : 0x0000ffffu) : 0u; });
    float lw = 0.f;
    u32 pp[32], Tq, lro, tgate;
    v4f acc[3] = {{0, 0, 0, 0}, {0, 0, 0, 0}, {0, 0, 0, 0}};
    sfor<32, 64>([&](auto I) {
      if constexpr (I < 36) {
        sfor<(I - 32) * 8, (I - 31) * 8>([&](auto N) { const float p = ex2((sc[N] - mw) * LOG2E); lw += p; pp[N] = pk(p, p); asm volatile("" : "+v"(pp[N])); });
        asm volatile("" : "+v"(lw));
      }
      if constexpr (I == 35) {
        lw += dpp<ROR8>(lw); lw = sum32(sum16(lw));
        *(v2u*)(lds + L_ML + sw * 8) = v2u{nosent(mw), nosent(lw)};
      }
      const v4u a = msk & pp[I - 32];
      sfor<0, 3>([&](auto P) { acc[P] = mfma32(a, KV[I][P], acc[P]); });
      if constexpr (I < 63) asm volatile("" : "+v"(acc[0]), "+v"(acc[1]), "+v"(acc[2]));
      PIN();
      sfor<issued_before(I), issued_before(I + 1)>(iss);
      if constexpr (I == GSTEP) { *(u32*)(lds + L_FP) = 1u; PIN(); }
      if constexpr (I == 50) {
        hop1_setup();
        Tq = tag_of(e);
        lro = (g >> 1) == (col >> 3) ? (u32)(L_RO + ((2 * sw + (g >> 1)) * 192 + c * 8 + 4 * (g & 1)) * 4) : (u32)(L_SP + l * 16);
        asm volatile("" : "+v"(Tq), "+v"(lro));
        PIN();
      }
      if constexpr (I == 60) {
        pace(line_end() + WOT - 15 * CC, tnow, IC(15));
        tgate = line_end() + TG;
        issWO(true);
      }
    });
    sfor<0, 3>([&](auto P) { *(v4f*)(lds + lro + P * 256) = acc[P]; });
    wg_barrier();
    if (tile) { __builtin_amdgcn_s_sleep(S1); hop1(Tq, tgate); }
    asm volatile(".Lcode_hi:");
    return;
  }

  v4u P0[10], P1[10], F2[8];
  {
    const rsrc_t rP = mk(pf ? wg : wq, D_ * pstr);
    sfor<0, 10>([&](auto I) { P0[I] = __builtin_amdgcn_raw_buffer_load_b128(rP, op, I * pstr, AUX_NT); __builtin_amdgcn_sched_barrier(0); });
    PIN();
  }
  setup();
  if (tid < 3) *(v4u*)(lds + L_ML + l * 16) = v4u{SENT, SENT, SENT, SENT};
  if (w == 1) { asm volatile("" :: "v"(e)); PIN(); *(u32*)(lds + L_TB + l * 4) = clk(); }
  wg_barrier();
  issWO(!pf);
  if (pf) sfor<0, 10>([&](auto I) { P1[I] = __builtin_amdgcn_raw_buffer_load_b128(mk(wu, D_ * F_ * 2), op, I * (F_ * 2), AUX_NT); });
  PIN();
  const u32 T = tag_of(e);
  {
    const u32 xd[10] = {xa[0], xa[1], xa[2], xa[3], xb[0], xb[1], xb[2], xb[3], xc[0], xc[1]};
    float s = 0.f;
    sfor<0, 10>([&](auto I) { s += lo16(xd[I]) + hi16(xd[I]); });
    const float mean = half_sum(s) * (1.f / D_);
    float v = 0.f;
    sfor<0, 10>([&](auto I) { const float d0 = lo16(xd[I]) - mean, d1 = hi16(xd[I]) - mean; v += d0 * d0 + d1 * d1; });
    const float var = half_sum(v) * (1.f / D_);
    const float mb = rbf(mean), inv = __builtin_amdgcn_rcpf(rbf(__builtin_sqrtf(rbf(rbf(var) + 1e-5f))));
    sfor<0, 5>([&](auto I) {
      const u32 d0 = pk(lo16(xd[2 * I]) - mb, hi16(xd[2 * I]) - mb), d1 = pk(lo16(xd[2 * I + 1]) - mb, hi16(xd[2 * I + 1]) - mb);
      *(v2u*)(lds + L_XN + w * 2560 + l * 40 + I * 8) = v2u{pk(lo16(d0) * inv, hi16(d0) * inv), pk(lo16(d1) * inv, hi16(d1) * inv)};
    });
    PIN();
  }
  sfor<0, 8>([&](auto I) { F2[I] = __builtin_amdgcn_raw_buffer_load_b128(mk(wd, F_ * D_ * 2), (u32)((64 * G + 8 * r) * (D_ * 2) + lc * 128 + c * 16) | ((w == 2 && tile) ? 0u : OOB), I * (D_ * 2), AUX_NT); });
  PIN();
  const u32 t0 = __builtin_amdgcn_readfirstlane(*(const u32*)(lds + L_TB));
  auto wait_clock = [&](int ticks) { while ((int)(clk() - t0) < ticks) __builtin_amdgcn_s_sleep(2); };
  const int ab = col >> 3, ar = 2 * g + ((col >> 2) & 1);
  hop1_setup();
  auto gemv = [&](u32 d, v4u W, v4f& lo, v4f& hi) { const v2u A = {d & m0, d & m1}; lo = mfma16(A, v2u{W[0], W[1]}, lo); hi = mfma16(A, v2u{W[2], W[3]}, hi); };
  const bool dv = (g & 1) == (col >> 3);
  const u32 ld0 = (u32)((g & 1) * 512 + (g >> 1) * 256 + c * 32);
  auto put = [&](int slot, v4f lo, v4f hi) { if (dv) { *(v4f*)(lds + L_RS + slot * 1024 + ld0) = lo; *(v4f*)(lds + L_RS + slot * 1024 + ld0 + 16) = hi; } };
  auto sum4 = [&](int slot) {
    v4f t[4];
    sfor<0, 4>([&](auto I) { t[I] = *(const v4f*)(lds + L_RS + (slot + I) * 1024 + l * 16); });
    v4f s = (t[0] + t[1]) + (t[2] + t[3]);
    sfor<0, 4>([&](auto E) { s[E.value] = sum32(s[E.value]); });
    return s;
  };
  {
    u32 xv[5];
    sfor<0, 5>([&](auto I) { xv[I] = *(const u32*)(lds + L_XN + w * 2560 + ab * 1280 + (320 * ks + 80 * w + 10 * ar) * 2 + I * 4); });
    v4f lo = {0, 0, 0, 0}, hi = {0, 0, 0, 0};
    sfor<0, 10>([&](auto I) { gemv(dup<I>(xv[I / 2]), P0[I], lo, hi); });
    put(w, lo, hi);
    if (pf) {
      lo = v4f{0, 0, 0, 0}; hi = v4f{0, 0, 0, 0};
      sfor<0, 10>([&](auto I) { gemv(dup<I>(xv[I / 2]), P1[I], lo, hi); });
      put(4 + w, lo, hi);
    }
    asm volatile("" ::: "memory");
    u32 n = 0;
    if (l == 0) n = __hip_atomic_fetch_add((u32*)(lds + L_CNT), 1u, __ATOMIC_RELAXED, __HIP_MEMORY_SCOPE_WORKGROUP);
    if (__builtin_amdgcn_readfirstlane(n) == SENT + 3u) {
      asm volatile("" ::: "memory");
      const u32 o = pf ? (u32)(WS_F + (pcol * 2 + ks) * 1024 + l * 16)
                       : (u32)(WS_Q + ks * 49152 + (l >> 4) * 24576 + pcol * 256 + (l & 15) * 16);
      const u32 om = o | ((l < 32 && pu != 12) ? 0u : OOB);
      __builtin_amdgcn_raw_buffer_store_b128(enc4n(sum4(0), T, l & 1), rW, om, 0, AUX_SC1);
      if (pf) __builtin_amdgcn_raw_buffer_store_b128(enc4n(sum4(4), T, l & 1), rW, om, 512, AUX_SC1);
    }
    PIN();
  }
  if (pf) issWO(true);
  const int me = h * 8 + b * 4 + sp;
  const bool fin = w == 0 && bid < 40;
  const int fq = (bid & 3) * 8 + c;
  const u32 ofin = (u32)(((bid >> 2) * 24 + r) * 512 + fq * 16);
  const u32 oidx = (u32)(((fq >> 4) * D_ + (bid >> 2) * 64 + (fq & 15) * 4) * 2);
  u32 oout = (fin && l < 8) ? oidx : OOB;
  v4f fw = {0, 0, 0, 0};
  float gate = 0.f, xv = 0.f;

  if (w == 3) {
    wait_clock(QT);
    const v4f s = gather2<49152, 48>(rW, (u32)(WS_Q + b * 24576 + h * 768 + l * 16), l, T);
    if (l < 48) *(v2u*)(lds + L_Q + l * 8) = v2u{pk(s[0], s[1]), pk(s[2], s[3])};
    asm volatile("" ::: "memory");
    if (l == 0) *(u32*)(lds + L_FQ) = 1u;
  } else if (w == 2) {
    if (tile) {
      wait_clock(FT);
      const v4f s = gather2<1024, 64>(rW, (u32)(WS_F + G * 2048 + l * 16), l, T);
      float hv[4];
      sfor<0, 4>([&](auto E) {
        const auto t = __builtin_amdgcn_permlane32_swap(asu(s[E.value]), asu(s[E.value]), false, false);
        const float gg = rbf(asf(t[0])), uu = rbf(asf(t[1]));
        const float z = 0.7978845608028654f * (gg + 0.044715f * gg * gg * gg);
        hv[E] = rbf(gg / (1.f + ex2(-2.f * LOG2E * z))) * uu;
      });
      if (l < 32) *(v2u*)(lds + L_H + l * 8) = v2u{pk(hv[0], hv[1]), pk(hv[2], hv[3])};
      const v4u hh = *(const v4u*)(lds + L_H + ab * 128 + ar * 16);
      v4f lo = {0, 0, 0, 0}, hi = {0, 0, 0, 0};
      sfor<0, 8>([&](auto I) { gemv(dup<I>(hh[I / 2]), F2[I], lo, hi); });
      put(8, lo, hi);
      v4f y = *(const v4f*)(lds + L_RS + 8 * 1024 + l * 16);
      sfor<0, 4>([&](auto E) { y[E.value] = sum32(y[E.value]); });
      __builtin_amdgcn_raw_buffer_store_b128(enc4(y, T), rW, l < 32 ? (u32)(WS_Y + (lc * 24 + G) * 512 + l * 16) : OOB, 0, AUX_SC1);
    }
  } else if (w == 1) {
    int base = 0, m1_ = 0, m2_ = 0;
    bool tilted = false;
    _Pragma("unroll 1") for (int k = 0; k < NCK; ++k) {
      const int S = CK0 + k * CKS, E = S + CKD, end = S < 32 ? 32 : 64, bS = __builtin_amdgcn_readlane(base, S);
      wait_clock(__builtin_amdgcn_readlane(base, E - RLEAD) + TOFF + (E - RLEAD) * TAU / 16);
      asm volatile("" ::: "memory");
      const v2u z = __builtin_amdgcn_raw_buffer_load_b64(rW, (u32)(WS_P + (k * 16 + col) * PSTR), 0, AUX_SC1);
      int v = (int)(z[0] - t0);
      if (z[1] != T || (u32)(v + 1000) > 2000u) v = bS + MISS;
      v += __builtin_amdgcn_mov_dpp(v, XOR1, 0xf, 0xf, true); v += __builtin_amdgcn_mov_dpp(v, XOR2, 0xf, 0xf, true);
      v += __builtin_amdgcn_mov_dpp(v, HMIR, 0xf, 0xf, true); v += __builtin_amdgcn_mov_dpp(v, ROR8, 0xf, 0xf, true);
      const int m = __builtin_amdgcn_readfirstlane(v) >> 4;
      if (S == 32) tilted = false;
      if (k == 0) base = l >= E ? m - (l > XS ? (l - XS) * XV / 16 : 0) : 0;
      else {
        const int bl = m - __builtin_amdgcn_readlane(base, E + 3 < 63 ? E + 3 : 63);
        int d = 0;
        if (k == 1) d = bl - KEEP1;
        else if (!tilted && (S < 32 || S >= 40) && m - m2_ >= (S == 12 ? GK12 : S == 16 ? GK16 : S < 32 ? GK : GV) && m - bS >= LV) {
          d = bl + GROW * (CKD < end - S ? CKD : end - S) - REST;
          const int n2 = (l < end ? l : end) - E;
          base += n2 > 0 ? n2 * (2 * SIG + (S < 32 ? 0 : XV)) / 16 : 0;
          tilted = true;
        } else if (bl > LMAX - (S > 32 ? (S - 32) * USL / 16 : 0)) d = bl - KEEP;
        const int rp = RAMP * (l - (E - 1));
        if (d > 0) base += rp < 0 ? 0 : rp < d ? rp : d;
      }
      m2_ = m1_; m1_ = m;
      *(u32*)(lds + L_TB + l * 4) = t0 + base;
    }
  } else {
    wait_flag(L_FQ, IC(16));
    xv = lo16(__builtin_amdgcn_raw_buffer_load_b16(rX, fin ? oidx + g * 2 : OOB, 0, 0));
    {
      const u32 z = __builtin_amdgcn_raw_buffer_load_b32(rW, l < 6 ? (u32)(me * REC + R_O + l * 128) : (l < 10 && tile) ? (u32)(WS_S + (lc * 24 + G) * 512 + (l - 6) * 128) : OOB, 0, AUX_SC1);
      asm volatile("" :: "v"(z));
    }
    __builtin_amdgcn_raw_buffer_store_b64(v2u{0u, 0u}, mk(out, 2 * D_ * 2), oout, 0, AUX_SC1);
    v2u mlw;
    _Pragma("unroll 1") for (int spin = 0; spin < SPIN_CAP * 4; ++spin) {
      asm volatile("" ::: "memory");
      mlw = *(const v2u*)(lds + L_ML + (l & 3) * 8);
      if (__all(mlw[0] != SENT && mlw[1] != SENT)) break;
      __builtin_amdgcn_s_sleep(16);
    }
    float Mg = asf(mlw[0]); Mg = fmaxf(Mg, dpp<XOR1>(Mg)); Mg = fmaxf(Mg, dpp<XOR2>(Mg));
    float Lg = asf(mlw[1]) * ex2((asf(mlw[0]) - Mg) * LOG2E); Lg += dpp<XOR1>(Lg); Lg += dpp<XOR2>(Lg);
    __builtin_amdgcn_raw_buffer_store_b128(enc4(v4f{Mg, Lg, 0.f, 0.f}, T), rW, l < 8 ? (u32)(me * REC + R_ML + l * 16) : OOB, 0, AUX_SC1);
    if (fin) {
      const float y = rbf(final_sum<16>(rW, ofin, WS_Y, T));
      gate = rbf(y / (1.f + ex2(-LOG2E * y)));
    }
    wait_flag(L_FP, IC(16));
    v4u ml4;
    SPIN {
      asm volatile("" ::: "memory");
      ml4 = __builtin_amdgcn_raw_buffer_load_b128(rW, (u32)((h * 8 + b * 4 + (l & 3)) * REC + R_ML + ((l >> 2) & 7) * 16), 0, AUX_SC1);
      if (__all(tag4(ml4) == T)) break;
      __builtin_amdgcn_s_sleep(16);
    }
    float M = dec(ml4[0]); M = fmaxf(M, dpp<XOR1>(M)); M = fmaxf(M, dpp<XOR2>(M));
    float L = dec(ml4[1]) * ex2((dec(ml4[0]) - M) * LOG2E); L += dpp<XOR1>(L); L += dpp<XOR2>(L);
    const float f = ex2((asf(mlw[0]) - M) * LOG2E) / L;
    fw = v4f{dpp<0x00>(f), dpp<0x55>(f), dpp<0xAA>(f), dpp<0xFF>(f)};
  }

  asm volatile("" :: "v"(cpf));
  __builtin_amdgcn_s_waitcnt(0x0F70);
  u32 ost = l < 48 ? (u32)(me * REC + R_O + l * 16) : OOB, oss = l < 32 ? (u32)(WS_S + (lc * 24 + G) * 512 + l * 16) : OOB;
  u32 oep = (bid == 0 && l == 0) ? (u32)WS_E : OOB;
  asm volatile("" : "+v"(ost), "+v"(oss), "+v"(oout), "+v"(oep), "+v"(xv), "+v"(fw), "+v"(gate));
  wg_barrier();
  if (w == 0) {
    v4f t[8];
    sfor<0, 8>([&](auto I) { t[I] = *(const v4f*)(lds + L_RO + I * 768 + l * 16); });
    __builtin_amdgcn_sched_barrier(0);
    const v4f s = ((t[0] + t[1]) * fw[0] + (t[2] + t[3]) * fw[1]) + ((t[4] + t[5]) * fw[2] + (t[6] + t[7]) * fw[3]);
    __builtin_amdgcn_raw_buffer_store_b128(enc4(s, T), rW, ost, 0, AUX_SC1);
  } else {
    __builtin_amdgcn_s_sleep(S1 - S0 - 1);
  }
  if (tile) {
    __builtin_amdgcn_s_sleep(S0);
    hop1(T, line_end() + TG);
    if (w == 0) {
      v4f t[8];
      sfor<0, 8>([&](auto I) { t[I] = *(const v4f*)(lds + L_RS + I * 1024 + l * 16); });
      __builtin_amdgcn_sched_barrier(0);
      v4f s = ((t[0] + t[1]) + (t[2] + t[3])) + ((t[4] + t[5]) + (t[6] + t[7]));
      sfor<0, 4>([&](auto E) { s[E.value] = sum32(s[E.value]); });
      __builtin_amdgcn_raw_buffer_store_b128(enc4(s, T), rW, oss, 0, AUX_SC1);
      if (fin) {
        __builtin_amdgcn_s_sleep(S2);
        const float o = xv + rbf(rbf(final_sum<0>(rW, ofin, WS_S, T)) * gate);
        const auto p2 = __builtin_amdgcn_permlane16_swap(asu(o), asu(o), false, false);
        const u32 d2 = pk(asf(p2[0]), asf(p2[1]));
        const auto p4 = __builtin_amdgcn_permlane32_swap(d2, d2, false, false);
        __builtin_amdgcn_raw_buffer_store_b64(v2u{p4[0], p4[1]}, mk(out, 2 * D_ * 2), oout, 0, AUX_SC1);
        __builtin_amdgcn_raw_buffer_store_b32(T, rW, oep, 0, AUX_SC1);
      }
    }
  }
}

extern "C" void kernel_launch(void* const* d_in, const int* in_sizes, int n_in, void* d_out, int out_size, void* d_ws, size_t ws_size,
                              hipStream_t stream) {
  fused_decode<<<256, 512, 0, stream>>>((const u16*)d_in[0], (const u16*)d_in[1], (const u16*)d_in[2], (const u16*)d_in[3], (const u16*)d_in[4],
                                        (const u16*)d_in[6], (const u16*)d_in[7], (const u16*)d_in[8], (u16*)d_out, (unsigned char*)d_ws);
}
```
